```python
import torch

def _softmax(x, axis=-1):
    return torch.softmax(x, dim=axis)
def _gelu(x):
    return torch.nn.functional.gelu(x, approximate="tanh")
def _silu(x):
    return torch.nn.functional.silu(x)
def _sigmoid(x):
    return torch.sigmoid(x)
def _layernorm(x, axis=-1, eps=1e-5):
    m = x.mean(dim=axis, keepdim=True)
    v = x.var(dim=axis, keepdim=True, unbiased=False)
    return (x - m) / torch.sqrt(v + eps)
def _scatter_add(tgt, idx, src, axis=0):
    return tgt.clone().index_add_(axis, idx, src)
def _segment_sum(src, seg, n):
    out = torch.zeros((n,) + src.shape[1:], dtype=src.dtype)
    return out.index_add_(0, seg, src)
def _pad(x, pw):
    flat = tuple(v for pair in reversed(pw) for v in pair)
    return torch.nn.functional.pad(x, flat)

kernel_name = 'parallel_sum__ssm_conv_h3__swiglu_ffn__prompt'

def setup_inputs():

    _w = (lambda a: (lambda t: t.to(torch.bfloat16) if t.is_floating_point() else t)(torch.as_tensor(a)))
    def _A_setup():
        B, S, D = 1, 2048, 640
        ks = 4
        return {
            "q": _w((lambda *s: torch.randn(*s, dtype=torch.bfloat16) * 0.05)(B, S, D)),
            "k": _w((lambda *s: torch.randn(*s, dtype=torch.bfloat16) * 0.8)(B, S, D)),
            "v": _w((lambda *s: torch.randn(*s, dtype=torch.bfloat16) * 0.4)(B, S, D)),
            "shift_k": _w((lambda *s: torch.randn(*s, dtype=torch.bfloat16) * 0.8)(D, ks) * 0.5),
            "ssm_k": _w((lambda *s: torch.randn(*s, dtype=torch.bfloat16) * 0.8)(D, S) * (1.0 / S ** 0.5)),
            "ssm_D": _w((lambda *s: torch.randn(*s, dtype=torch.bfloat16) * 0.4)(D)),
        }
    _A_in = _A_setup()

    def _B_setup():
        B, S, D, F = 1, 2048, 640, 1728
        return {
            "x": _w((lambda *s: torch.randn(*s, dtype=torch.bfloat16) * 0.05)(B, S, D)),
            "w_gate": _w((lambda *s: torch.randn(*s, dtype=torch.bfloat16) * 0.05)(D, F)),
            "w_up": _w((lambda *s: torch.randn(*s, dtype=torch.bfloat16) * 0.05)(D, F)),
            "w_down": _w((lambda *s: torch.randn(*s, dtype=torch.bfloat16) * 0.05)(F, D)),
        }
    _B_in = _B_setup()

    return {
        "A_q": _A_in["q"],
        "A_k": _A_in["k"],
        "A_v": _A_in["v"],
        "A_shift_k": _A_in["shift_k"],
        "A_ssm_k": _A_in["ssm_k"],
        "A_ssm_D": _A_in["ssm_D"],
        "B_x": _B_in["x"],
        "B_w_gate": _B_in["w_gate"],
        "B_w_up": _B_in["w_up"],
        "B_w_down": _B_in["w_down"],
    }

def reference(A_q, A_k, A_v, A_shift_k, A_ssm_k, A_ssm_D, B_x, B_w_gate, B_w_up, B_w_down):
    def _A_ref(q, k, v, shift_k, ssm_k, ssm_D):
        B, S, D = q.shape
        ks = shift_k.shape[-1]
        k_pad = _pad(k, ((0, 0), (ks - 1, 0), (0, 0)))
        k_shift = torch.zeros_like(k)
        for t in range(ks):
            k_shift = k_shift + k_pad[:, t:t + S, :] * shift_k[:, ks - 1 - t][None, None, :]
        kv = k_shift * v
        L = 2 * S
        kv_pad = _pad(kv, ((0, 0), (0, S), (0, 0)))
        kf_pad = _pad(ssm_k, ((0, 0), (0, S)))
        kv_f = (lambda x, axes=(-2,-1): torch.fft.rfft2(x.float(), dim=axes))(kv_pad[:, None, :, :], axes=(1, 2))
        kf_f = (lambda x, axes=(-2,-1): torch.fft.rfft2(x.float(), dim=axes))(kf_pad[:, None, :], axes=(1, 2))
        kf_f = (lambda x, *d: x.permute(*d))(kf_f, 1, 2, 0)
        y_f = kv_f * kf_f[None, :, :, :]
        y = (lambda x, s, axes=(-2,-1): torch.fft.irfft2(x, s=s, dim=axes).to(torch.bfloat16))(y_f, (1, L), axes=(1, 2))[:, 0, :S, :]
        y = y + kv * ssm_D[None, None, :]
        return q * y

    def _B_ref(x, w_gate, w_up, w_down):
        g = _silu(x @ w_gate)
        u = x @ w_up
        return (g * u) @ w_down

    x = A_q
    xn = _layernorm(x, axis=-1)
    return x + _A_ref(xn, A_k, A_v, A_shift_k, A_ssm_k, A_ssm_D) + _B_ref(xn, B_w_gate, B_w_up, B_w_down)
```

```cpp
#include <hip/hip_runtime.h>
#include <hip/hip_bf16.h>
#include <stdint.h>
typedef unsigned short u16;
constexpr int S=2048, D=640, F=1728;
__device__ __forceinline__ float bf2f(u16 x){ return __uint_as_float(((unsigned)x)<<16); }
__device__ __forceinline__ u16 f2bf(float f){ __hip_bfloat16 h = __float2bfloat16(f); return *(u16*)&h; }
__device__ __forceinline__ float rbf(float f){ return bf2f(f2bf(f)); }

__global__ void k_ln(const u16* x, u16* xn){
  int row=blockIdx.x; __shared__ float red[256];
  float s=0; for(int i=threadIdx.x;i<D;i+=256) s+=bf2f(x[row*D+i]);
  red[threadIdx.x]=s; __syncthreads();
  for(int o=128;o>0;o>>=1){ if(threadIdx.x<o) red[threadIdx.x]+=red[threadIdx.x+o]; __syncthreads(); }
  float mean=red[0]/D; __syncthreads();
  float q=0; for(int i=threadIdx.x;i<D;i+=256){ float d=bf2f(x[row*D+i])-mean; q+=d*d; }
  red[threadIdx.x]=q; __syncthreads();
  for(int o=128;o>0;o>>=1){ if(threadIdx.x<o) red[threadIdx.x]+=red[threadIdx.x+o]; __syncthreads(); }
  float var=rbf(red[0]/D); float m=rbf(mean);
  float den=rbf(sqrtf(rbf(var+1e-5f)));
  for(int i=threadIdx.x;i<D;i+=256){ float xc=rbf(bf2f(x[row*D+i])-m); xn[row*D+i]=f2bf(xc/den); }
}
__global__ void k_kv(const u16* k,const u16* v,const u16* sk,u16* kv){
  int i=blockIdx.x*256+threadIdx.x; if(i>=S*D) return; int t=i/D,d=i%D;
  float acc=0.f;
  for(int tt=0;tt<4;++tt){ int j=3-tt; int s=t-j; float kk= s>=0? bf2f(k[s*D+d]):0.f; acc=rbf(acc+rbf(kk*bf2f(sk[d*4+j]))); }
  kv[i]=f2bf(acc*bf2f(v[i]));
}
__global__ void k_conv(const u16* kv,const u16* h,const u16* Dv,const u16* xn,u16* A){
  int i=blockIdx.x*256+threadIdx.x; if(i>=S*D) return; int t=i/D,d=i%D;
  float acc=0.f; for(int s=0;s<=t;++s) acc+=bf2f(kv[s*D+d])*bf2f(h[d*S+t-s]);
  float y=rbf(acc); y=rbf(y+rbf(bf2f(kv[i])*bf2f(Dv[d])));
  A[i]=f2bf(bf2f(xn[i])*y);
}
__global__ void k_g1(const u16* xn,const u16* wg,const u16* wu,u16* hh){
  int i=blockIdx.x*256+threadIdx.x; if(i>=S*F) return; int t=i/F,f=i%F;
  float g=0,u=0; for(int k=0;k<D;++k){ float a=bf2f(xn[t*D+k]); g+=a*bf2f(wg[k*F+f]); u+=a*bf2f(wu[k*F+f]); }
  g=rbf(g);u=rbf(u); float sg=rbf(g/(1.f+expf(-g))); hh[i]=f2bf(sg*u);
}
__global__ void k_g2(const u16* hh,const u16* wd,const u16* x,const u16* A,u16* out){
  int i=blockIdx.x*256+threadIdx.x; if(i>=S*D) return; int t=i/D,d=i%D;
  float a=0; for(int f=0;f<F;++f) a+=bf2f(hh[t*F+f])*bf2f(wd[f*D+d]);
  out[i]=f2bf(rbf(bf2f(x[i])+bf2f(A[i]))+rbf(a));
}
extern "C" void kernel_launch(void* const* d_in, const int* in_sizes, int n_in, void* d_out, int out_size, void* d_ws, size_t ws_size, hipStream_t stream){
  const u16* q=(const u16*)d_in[0]; const u16* k=(const u16*)d_in[1]; const u16* v=(const u16*)d_in[2];
  const u16* sk=(const u16*)d_in[3]; const u16* h=(const u16*)d_in[4]; const u16* Dv=(const u16*)d_in[5];
  const u16* wg=(const u16*)d_in[7]; const u16* wu=(const u16*)d_in[8]; const u16* wd=(const u16*)d_in[9];
  u16* xn=(u16*)d_ws; u16* kv=xn+S*D; u16* A=kv+S*D; u16* hh=A+S*D;
  k_ln<<<S,256,0,stream>>>(q,xn);
  k_kv<<<S*D/256,256,0,stream>>>(k,v,sk,kv);
  k_conv<<<S*D/256,256,0,stream>>>(kv,h,Dv,xn,A);
  k_g1<<<S*F/256,256,0,stream>>>(xn,wg,wu,hh);
  k_g2<<<S*D/256,256,0,stream>>>(hh,wd,q,A,(u16*)d_out);
}
```

```cpp
#include <hip/hip_runtime.h>
#include <hip/hip_bf16.h>
#include <stdint.h>
#define LAS __attribute__((address_space(3)))
#define DI __device__ __forceinline__
typedef unsigned short u16;
typedef short s16x4 __attribute__((ext_vector_type(4)));
typedef short s16x8 __attribute__((ext_vector_type(8)));
typedef __bf16 bf16x2 __attribute__((ext_vector_type(2)));
typedef float f32x2 __attribute__((ext_vector_type(2)));
typedef float f32x4 __attribute__((ext_vector_type(4)));
typedef float f32x16 __attribute__((ext_vector_type(16)));
typedef unsigned u32x4 __attribute__((ext_vector_type(4)));
typedef unsigned u32x2 __attribute__((ext_vector_type(2)));
constexpr int S_=2048, D_=640, F_=1728;
#define GAP() __builtin_amdgcn_sched_barrier(0)
DI unsigned rfl(unsigned x){ return __builtin_amdgcn_readfirstlane(x); }
DI u32x4 mk_rsrc(const void* p, unsigned bytes){
  unsigned long long a=(unsigned long long)p;
  u32x4 r; r[0]=rfl((unsigned)a); r[1]=rfl((unsigned)(a>>32))&0xffffu; r[2]=bytes; r[3]=0x00020000u; return r;
}
#define SETM0(x) asm volatile("s_mov_b32 m0, %0\n\ts_nop 0"::"s"(x))
#define DMA(voff,rsrc,soff,imm) asm volatile("buffer_load_dwordx4 %0, %1, %2 offen offset:" #imm " lds"::"v"(voff),"s"(rsrc),"s"(soff))
#define WAITV(n) asm volatile("s_waitcnt vmcnt(" #n ")":::"memory")
#define BARRIER() asm volatile("s_waitcnt lgkmcnt(0)\n\ts_barrier":::"memory")
#define MFMA32(acc,a,b) asm volatile("v_mfma_f32_32x32x16_bf16 %0, %1, %2, %0":"+v"(acc):"v"(a),"v"(b))
#define MFMA32Z(acc,a,b) asm volatile("v_mfma_f32_32x32x16_bf16 %0, %1, %2, 0":"=&v"(acc):"v"(a),"v"(b))
#define MFMA16(acc,a,b) asm volatile("v_mfma_f32_16x16x32_bf16 %0, %1, %2, %0":"+v"(acc):"v"(a),"v"(b))
#define MFMA16Z(acc,a,b) asm volatile("v_mfma_f32_16x16x32_bf16 %0, %1, %2, 0":"=&v"(acc):"v"(a),"v"(b))
DI float bf2f(unsigned x16){ return __uint_as_float(x16<<16); }
DI float bflo(unsigned x){ return __uint_as_float(x<<16); }
DI float bfhi(unsigned x){ return __uint_as_float(x&0xffff0000u); }
DI unsigned cvtpk(float lo,float hi){ f32x2 v={lo,hi}; bf16x2 b=__builtin_convertvector(v,bf16x2); return __builtin_bit_cast(unsigned,b); }
DI float rbf(float x){ return bflo(cvtpk(x,x)); }
DI u32x4 cat(s16x4 a, s16x4 b){ s16x8 r=__builtin_shufflevector(a,b,0,1,2,3,4,5,6,7); return __builtin_bit_cast(u32x4,r); }
DI u32x4 cat2(u32x2 a,u32x2 b){ return __builtin_shufflevector(a,b,0,1,2,3); }
DI s16x4 trread(const char* p){ return __builtin_amdgcn_ds_read_tr16_b64_v4i16((LAS s16x4*)p); }

constexpr size_t WS_XN=0, WS_ST=WS_XN+S_*D_*2, WS_UT=WS_ST+16384, WS_WDF=WS_UT+D_*S_*2, WS_HF=WS_WDF+F_*D_*2, WS_YT=WS_HF+64*108*1024, WS_END=WS_YT+D_*S_*2;

DI float wsum32(float v){
  v+=__shfl_xor(v,1); v+=__shfl_xor(v,2); v+=__shfl_xor(v,4); v+=__shfl_xor(v,8); v+=__shfl_xor(v,16); return v; }
__global__ __launch_bounds__(256) void k_prep(const u16* __restrict__ x,const u16* __restrict__ kk,const u16* __restrict__ vv,const u16* __restrict__ sk,const u16* __restrict__ wd,char* ws){
  extern __shared__ __attribute__((aligned(16))) char smem[];
  const int tid=threadIdx.x, lane=tid&63; const unsigned w=rfl(tid>>6); const int b=blockIdx.x;
  {
    const int row=8*b+2*w+(lane>>5), l=lane&31;
    const u32x4* xr=(const u32x4*)(x+row*D_);
    u32x4 c[3]; c[0]=xr[l]; c[1]=xr[l+32]; c[2]= l<16? xr[l+64] : u32x4{0,0,0,0};
    float f[24];
    #pragma unroll
    for(int i=0;i<3;i++) for(int e=0;e<4;e++){ f[8*i+2*e]=bflo(c[i][e]); f[8*i+2*e+1]=bfhi(c[i][e]); }
    float s=0; for(int i=0;i<24;i++) s+=f[i];
    const float mean=wsum32(s)*(1.f/640.f);
    float q=0;
    #pragma unroll
    for(int i=0;i<24;i++){ float d=f[i]-mean; if(i>=16 && l>=16) d=0.f; q+=d*d; }
    const float var=rbf(wsum32(q)*(1.f/640.f)); const float m=rbf(mean);
    const float den=rbf(sqrtf(rbf(var+1e-5f))); const float rd=1.f/den;
    u32x4* xo=(u32x4*)((u16*)(ws+WS_XN)+row*D_);
    #pragma unroll
    for(int i=0;i<3;i++){ u32x4 o; for(int e=0;e<4;e++) o[e]=cvtpk(rbf(f[8*i+2*e]-m)*rd, rbf(f[8*i+2*e+1]-m)*rd);
      if(i<2||l<16) xo[l+32*i]=o; }
    if(l==0) ((unsigned*)(ws+WS_ST))[row]=(cvtpk(m,den));
  }
  if(b<160){
    const int tb=b/5, cb=b%5, t0=tb*64, c=tid&15, tg=tid>>4; const int d0=cb*128+c*8;
    u32x4 kr[7], vr[4], sw[4];
    #pragma unroll
    for(int i=0;i<7;i++){ int t=t0+4*tg-3+i; kr[i]= t>=0? *(const u32x4*)(kk+(size_t)t*D_+d0) : u32x4{0,0,0,0}; }
    #pragma unroll
    for(int i=0;i<4;i++) vr[i]=*(const u32x4*)(vv+(size_t)(t0+4*tg+i)*D_+d0);
    #pragma unroll
    for(int i=0;i<4;i++) sw[i]=*(const u32x4*)(sk+d0*4+i*8);
    #pragma unroll
    for(int e=0;e<8;e++){
      const unsigned a=sw[e>>1][(e&1)*2], bb=sw[e>>1][(e&1)*2+1];
      const float w0=bflo(a),w1=bfhi(a),w2=bflo(bb),w3=bfhi(bb);
      float kf[7];
      #pragma unroll
      for(int i=0;i<7;i++) kf[i]=(e&1)? bfhi(kr[i][e>>1]) : bflo(kr[i][e>>1]);
      float o[4];
      #pragma unroll
      for(int i=0;i<4;i++){ float ks=kf[i]*w3; ks=fmaf(kf[i+1],w2,ks); ks=fmaf(kf[i+2],w1,ks); ks=fmaf(kf[i+3],w0,ks);
        const float vf=(e&1)? bfhi(vr[i][e>>1]) : bflo(vr[i][e>>1]); o[i]=ks*vf; }
      u32x2 pk={cvtpk(o[3],o[2]),cvtpk(o[1],o[0])};
      const int slot=(15-tg)^c;
      *(u32x2*)(smem+(8*c+e)*128+slot*8)=pk;
    }
    __syncthreads();
    #pragma unroll
    for(int i=0;i<4;i++){ const int id=tid+256*i, r=id>>3, ch=id&7, cc=r>>3;
      u32x4 v=*(const u32x4*)(smem+r*128+((ch^(cc>>1))*16));
      if(cc&1) v=u32x4{v[2],v[3],v[0],v[1]};
      *(u32x4*)((u16*)(ws+WS_UT)+(size_t)(cb*128+r)*S_+(S_-64-t0)+ch*8)=v; }
    __syncthreads();
  }
  const int gw=4*b+w;
  if(gw<540){
    const int s=gw/10, c64=gw%10;
    char* L=smem+16384+w*4096;
    #pragma unroll
    for(int i=0;i<4;i++){ const int r=8*i+(lane>>3);
      u32x4 v=*(const u32x4*)(wd+(size_t)(32*s+r)*D_+c64*64+(lane&7)*8);
      *(u32x4*)(L+r*128+(((lane&7)^((r>>2)&7))*16))=v; }
    asm volatile("s_waitcnt lgkmcnt(0)":::"memory");
    const int g=lane>>4, q=(lane&15)>>2, p=lane&3;
    #pragma unroll
    for(int f4=0;f4<4;f4++){
      s16x4 h[2];
      #pragma unroll
      for(int t=0;t<2;t++){ const int r=8*g+4*t+q; const int ch=2*f4+(p>>1);
        h[t]=trread(L+r*128+((ch^((r>>2)&7))*16)+(p&1)*8); }
      const int D16=c64*4+f4, nt=D16/5, dt=D16%5;
      *(u32x4*)(ws+WS_WDF+((size_t)((nt*54+s)*5+dt)*64+lane)*16)=cat(h[0],h[1]);
    }
  }
}

constexpr int G1_XB=16384, G1_GB=14336, G1_STG=G1_XB+G1_GB+128+G1_GB;
struct G1Frag { u32x4 w[4]; u32x4 x[2]; };
__global__ __launch_bounds__(256,1) void k_gemm1(const u16* __restrict__ wg,const u16* __restrict__ wu,char* ws){
  extern __shared__ __attribute__((aligned(16))) char smem[];
  const u16* xn=(const u16*)(ws+WS_XN); u16* hf=(u16*)(ws+WS_HF);
  const int bid=blockIdx.x;
  const int tid=threadIdx.x, lane=tid&63; const unsigned w=rfl(tid>>6);
  const int xcd=bid&7, jj=bid>>3; const int mi=(xcd>>2)*8+(jj&7), ni=(xcd&3)*4+(jj>>3);
  const u32x4 rx=mk_rsrc(xn,S_*D_*2); const u32x4 rw=mk_rsrc(w<2?wg:wu, D_*F_*2);
  const unsigned lds0=(unsigned)(size_t)(LAS char*)smem;
  unsigned vx[4], vw[7];
  #pragma unroll
  for(int i=0;i<4;i++){ int row=32*w+8*i+(lane>>3); int c=(lane&7)^((row>>1)&7); vx[i]=(128*mi+row)*1280+c*16-1024*i; }
  #pragma unroll
  for(int i=0;i<7;i++){ int q=(w&1)*7+i; int c=64*q+lane; int k=c/14, cc=c-14*k; vw[i]=k*3456+ni*224+cc*16-1024*(i&3); }
  const unsigned m0x=lds0+w*4096, m0w=lds0+G1_XB+(w>>1)*(G1_GB+128)+(w&1)*7168;
  auto issue_x=[&](int s,int buf){ unsigned so=s*128; SETM0(m0x+buf*G1_STG); DMA(vx[0],rx,so,0); DMA(vx[1],rx,so,1024); DMA(vx[2],rx,so,2048); DMA(vx[3],rx,so,3072); };
  auto issue_w0=[&](int s,int buf){ unsigned so=s*221184; SETM0(m0w+buf*G1_STG); DMA(vw[0],rw,so,0); DMA(vw[1],rw,so,1024); DMA(vw[2],rw,so,2048); DMA(vw[3],rw,so,3072); };
  auto issue_w1=[&](int s,int buf){ unsigned so=s*221184; SETM0(m0w+buf*G1_STG+4096); DMA(vw[4],rw,so,0); DMA(vw[5],rw,so,1024); DMA(vw[6],rw,so,2048); };
  const int wm=w>>1, wn=w&1;
  int xa[4];
  #pragma unroll
  for(int j=0;j<4;j++) xa[j]=(64*wm+32*wn+(lane&31))*128+(((2*j+(lane>>5))^((lane>>1)&7))*16);
  const int xo=wn? -4096:4096;
  const int pperm=((lane&1)<<1|((lane>>1)&1));
  const int wa=G1_XB+((lane>>4)&1)*(G1_GB+128)+(8*(lane>>5)+((lane&15)>>2))*224+pperm*8;
  const int wt0=wn*4;
  f32x16 acc[7];
  auto ldw=[&](int buf,int j,int tile)->u32x4{ const char* p=smem+buf*G1_STG+wa+(16*j)*224+tile*32; return cat(trread(p),trread(p+4*224)); };
  auto ldx=[&](int buf,int j,int ms)->u32x4{ return *(const u32x4*)(smem+buf*G1_STG+xa[j]+(ms? xo:0)); };
  G1Frag fr[2];
  issue_x(0,0); issue_w0(0,0); issue_w1(0,0);
  issue_x(1,1); issue_w0(1,1); issue_w1(1,1);
  WAITV(11); BARRIER();
  fr[0].x[0]=ldx(0,0,0); fr[0].x[1]=ldx(0,0,1);
  #pragma unroll
  for(int t=0;t<3;t++) fr[0].w[t]=ldw(0,0,wt0+t);
  fr[0].w[3]=ldw(0,0,3);
  GAP();
  #pragma unroll
  for(int s=0;s<10;s++){
    const int buf=s%3;
    #pragma unroll
    for(int j=0;j<4;j++){
      G1Frag& c=fr[j&1]; G1Frag& n=fr[(j&1)^1];
      const bool has_next = !(s==9 && j==3);
      const int nb = j==3 ? (s+1)%3 : buf, nj=(j+1)&3;
      if(j==3 && s<9){ if(s<=7) WAITV(11); else WAITV(0); BARRIER(); }
      #define MF(t,wi,xi) do{ if(s==0&&j==0) MFMA32Z(acc[t],c.w[wi],c.x[xi]); else MFMA32(acc[t],c.w[wi],c.x[xi]); }while(0)
      MF(0,0,0); if(has_next){ n.x[0]=ldx(nb,nj,0); n.w[0]=ldw(nb,nj,wt0+0);} GAP();
      MF(3,0,1); if(has_next){ n.w[1]=ldw(nb,nj,wt0+1);} GAP();
      MF(1,1,0); if(has_next){ n.x[1]=ldx(nb,nj,1); n.w[2]=ldw(nb,nj,wt0+2);} GAP();
      MF(4,1,1); if(has_next){ n.w[3]=ldw(nb,nj,3);} GAP();
      MF(2,2,0);
      if(s==0){ if(j==0) issue_x(2,2); if(j==1) issue_w0(2,2); if(j==2) issue_w1(2,2); }
      if(j==3 && s+3<10) issue_x(s+3,buf);
      if(j==0 && s>=1 && s+2<10) issue_w0(s+2,(s+2)%3);
      if(j==1 && s>=1 && s+2<10) issue_w1(s+2,(s+2)%3);
      GAP();
      MF(5,2,1); GAP();
      MF(6,3,0); GAP();
    }
  }
  asm volatile("s_nop 15":"+v"(acc[0]),"+v"(acc[1]),"+v"(acc[2]),"+v"(acc[3]),"+v"(acc[4]),"+v"(acc[5]),"+v"(acc[6]));
  const float L2E=1.4426950408889634f;
  #pragma unroll
  for(int t=0;t<7;t++){
    const int tile = t==6?3: wt0+(t%3); const int ms = (t==6||t<3)? wn : 1-wn;
    const int ft=ni*7+tile;
    unsigned o[4];
    #pragma unroll
    for(int e=0;e<4;e++){
      f32x2 g={acc[t][2*e],acc[t][2*e+1]}, u={acc[t][8+2*e],acc[t][8+2*e+1]};
      f32x2 s=g*(-L2E); f32x2 ex={__builtin_amdgcn_exp2f(s[0]),__builtin_amdgcn_exp2f(s[1])};
      f32x2 d=ex+1.f; f32x2 rc={__builtin_amdgcn_rcpf(d[0]),__builtin_amdgcn_rcpf(d[1])};
      f32x2 h=(g*u)*rc; o[e]=cvtpk(h[0],h[1]);
    }
    if(ft<108){
      const int mb=mi*4+wm*2+ms;
      u32x4 v={o[0],o[1],o[2],o[3]};
      *(u32x4*)(hf+((size_t)(mb*108+ft)*64+lane)*8)=v;
    }
  }
}

constexpr int CV_HC=4160, CV_H=4*CV_HC, CV_U=4608, CV_CH=CV_H+CV_U;
__global__ __launch_bounds__(256,1) void k_conv(const u16* __restrict__ hk,const u16* __restrict__ Dv,char* ws){
  extern __shared__ __attribute__((aligned(16))) char smem[];
  const int tid=threadIdx.x, lane=tid&63; const unsigned w=rfl(tid>>6);
  const int d=4*blockIdx.x+w;
  char* L=smem+w*CV_CH; const unsigned ldsL=(unsigned)(size_t)(LAS char*)smem+w*CV_CH;
  const u32x4 rh=mk_rsrc(hk,D_*S_*2);
  #pragma unroll
  for(int c=0;c<4;c++){
    unsigned vo=lane*16+2*c; unsigned so=d*4096;
    SETM0(ldsL+c*CV_HC+32); DMA(vo,rh,so,0); DMA(vo,rh,so,1024); DMA(vo,rh,so,2048); DMA(vo,rh,so,3072);
  }
  { const int c=lane>>4,p=lane&15; u16 v= p>=16-c? hk[d*S_+p-16+c] : (u16)0; *(u16*)(L+c*CV_HC+p*2)=v; }
  const u32x4* ug=(const u32x4*)((const u16*)(ws+WS_UT)+(size_t)d*S_);
  #pragma unroll
  for(int i=0;i<4;i++) *(u32x4*)(L+CV_H+(lane+64*i)*16)=ug[lane+64*i];
  if(lane<32) *(u32x4*)(L+CV_H+4096+lane*16)=u32x4{0,0,0,0};
  const float Dd=bf2f(Dv[d]);
  WAITV(0); asm volatile("s_waitcnt lgkmcnt(0)":::"memory");
  const int i=lane&15,q=lane>>4,c=(i+1)&3;
  const char* pa=L+c*CV_HC+2*(1+i-c+8*q);
  const char* pb=L+CV_H+2*(2032-16*i+8*q);
  f32x4 acc[8];
  u32x4 B[2][8]; u32x4 A[2];
  auto ldA=[&](int rho,int a)->u32x4{ const char* p=pa+64*rho+512*a; return cat2(*(const u32x2*)p,*(const u32x2*)(p+8)); };
  auto ldB=[&](int rho,int b)->u32x4{ return *(const u32x4*)(pb+64*rho-512*b); };
  #pragma unroll
  for(int b=0;b<8;b++) B[0][b]=ldB(0,b);
  A[0]=ldA(0,7);
  GAP();
  #pragma unroll
  for(int rho=0;rho<8;rho++){
    #pragma unroll
    for(int aa=0;aa<8;aa++){ const int a=7-aa; const int cur=(rho*8+aa)&1;
      if(aa<7) A[cur^1]=ldA(rho,a-1); else if(rho<7) A[cur^1]=ldA(rho+1,7);
      if(rho<7) B[(rho&1)^1][aa]=ldB(rho+1,aa);
      #pragma unroll
      for(int nu=a;nu<8;nu++){
        if(rho==0 && nu==a) MFMA16Z(acc[nu],A[cur],B[rho&1][nu-a]); else MFMA16(acc[nu],A[cur],B[rho&1][nu-a]);
      }
      GAP();
    }
  }
  asm volatile("s_nop 15":"+v"(acc[0]),"+v"(acc[1]),"+v"(acc[2]),"+v"(acc[3]),"+v"(acc[4]),"+v"(acc[5]),"+v"(acc[6]),"+v"(acc[7]));
  u16* yt=(u16*)(ws+WS_YT)+(size_t)d*S_;
  unsigned pk[8][2];
  #pragma unroll
  for(int nu=0;nu<8;nu++){ const int t=256*nu+16*i+4*q;
    u32x2 uu=*(const u32x2*)(L+CV_H+2*(2044-t));
    float y0=fmaf(bfhi(uu[1]),Dd,acc[nu][0]), y1=fmaf(bflo(uu[1]),Dd,acc[nu][1]), y2=fmaf(bfhi(uu[0]),Dd,acc[nu][2]), y3=fmaf(bflo(uu[0]),Dd,acc[nu][3]);
    pk[nu][0]=cvtpk(y0,y1); pk[nu][1]=cvtpk(y2,y3); }
  #pragma unroll
  for(int nu=0;nu<8;nu+=2){
    auto r0=__builtin_amdgcn_permlane16_swap(pk[nu][0],pk[nu+1][0],false,false);
    auto r1=__builtin_amdgcn_permlane16_swap(pk[nu][1],pk[nu+1][1],false,false);
    u32x4 v={r0[0],r1[0],r0[1],r1[1]};
    const int t=256*(nu+(q&1))+16*i+8*(q>>1);
    *(u32x4*)(yt+t)=v;
  }
}

constexpr int G2_Y=81920;
__global__ __launch_bounds__(256,1) void k_gemm2(const u16* __restrict__ x,char* ws,u16* __restrict__ out){
  extern __shared__ __attribute__((aligned(16))) char smem[];
  const int tid=threadIdx.x, lane=tid&63; const unsigned w=rfl(tid>>6); const int bid=blockIdx.x;
  const int xcd=bid&7, jj=bid>>3; const int mt=(xcd>>1)*8+(jj&7), nt=(xcd&1)*4+(jj>>3);
  const u32x4 rh=mk_rsrc(ws+WS_HF,64*108*1024), rwd=mk_rsrc(ws+WS_WDF,F_*D_*2);
  const int j=lane&15,q=lane>>4;
  const unsigned vh0=((q>>1)*64+(q&1)*32+j)*16, vh1=vh0+108*1024;
  const unsigned vw0=lane*16, vw1=lane*16+4096;
  { const u16* yt=(const u16*)(ws+WS_YT);
    #pragma unroll
    for(int i=0;i<3;i++){ int id=tid+256*i; if(id<640){ int r=id>>3,ch=id&7; u32x4 v=*(const u32x4*)(yt+(size_t)(80*nt+r)*S_+64*mt+ch*8); *(u32x4*)(smem+G2_Y+r*160+ch*16)=v; } } }
  const int trow=64*mt+16*w+j;
  u32x2 xv[5];
  #pragma unroll
  for(int dt=0;dt<5;dt++) xv[dt]=*(const u32x2*)(x+(size_t)trow*D_+80*nt+16*dt+4*q);
  const unsigned stv=((const unsigned*)(ws+WS_ST))[trow];
  f32x4 acc[4][5];
  u32x4 H[14][4], W[14][5];
  auto ld=[&](int i){ const int s=14*w+i; const bool ok=s<54;
    const unsigned sh= ok? (unsigned)((2*mt*108+2*s)*1024) : 0x10000000u; const unsigned sw= ok? (unsigned)(((nt*54+s)*5)*1024) : 0x10000000u;
    asm volatile("buffer_load_dwordx4 %0, %1, %2, %3 offen":"=v"(W[i][0]):"v"(vw0),"s"(rwd),"s"(sw));
    asm volatile("buffer_load_dwordx4 %0, %1, %2, %3 offen":"=v"(H[i][0]):"v"(vh0),"s"(rh),"s"(sh));
    asm volatile("buffer_load_dwordx4 %0, %1, %2, %3 offen offset:256":"=v"(H[i][1]):"v"(vh0),"s"(rh),"s"(sh));
    asm volatile("buffer_load_dwordx4 %0, %1, %2, %3 offen":"=v"(H[i][2]):"v"(vh1),"s"(rh),"s"(sh));
    asm volatile("buffer_load_dwordx4 %0, %1, %2, %3 offen offset:256":"=v"(H[i][3]):"v"(vh1),"s"(rh),"s"(sh));
    asm volatile("buffer_load_dwordx4 %0, %1, %2, %3 offen offset:1024":"=v"(W[i][1]):"v"(vw0),"s"(rwd),"s"(sw));
    asm volatile("buffer_load_dwordx4 %0, %1, %2, %3 offen offset:2048":"=v"(W[i][2]):"v"(vw0),"s"(rwd),"s"(sw));
    asm volatile("buffer_load_dwordx4 %0, %1, %2, %3 offen offset:3072":"=v"(W[i][3]):"v"(vw0),"s"(rwd),"s"(sw));
    asm volatile("buffer_load_dwordx4 %0, %1, %2, %3 offen":"=v"(W[i][4]):"v"(vw1),"s"(rwd),"s"(sw));
  };
  GAP();
  ld(0); ld(1); ld(2);
  #pragma unroll
  for(int i=0;i<14;i++){
    if(i+3<14){ ld(i+3); asm volatile("s_waitcnt vmcnt(27)":"+v"(H[i][0]),"+v"(H[i][1]),"+v"(H[i][2]),"+v"(H[i][3]),"+v"(W[i][0]),"+v"(W[i][1]),"+v"(W[i][2]),"+v"(W[i][3]),"+v"(W[i][4])); }
    else if(i==11) asm volatile("s_waitcnt vmcnt(18)":"+v"(H[i][0]),"+v"(H[i][1]),"+v"(H[i][2]),"+v"(H[i][3]),"+v"(W[i][0]),"+v"(W[i][1]),"+v"(W[i][2]),"+v"(W[i][3]),"+v"(W[i][4]));
    else if(i==12) asm volatile("s_waitcnt vmcnt(9)":"+v"(H[i][0]),"+v"(H[i][1]),"+v"(H[i][2]),"+v"(H[i][3]),"+v"(W[i][0]),"+v"(W[i][1]),"+v"(W[i][2]),"+v"(W[i][3]),"+v"(W[i][4]));
    else asm volatile("s_waitcnt vmcnt(0)":"+v"(H[i][0]),"+v"(H[i][1]),"+v"(H[i][2]),"+v"(H[i][3]),"+v"(W[i][0]),"+v"(W[i][1]),"+v"(W[i][2]),"+v"(W[i][3]),"+v"(W[i][4]));
    GAP();
    #pragma unroll
    for(int tt=0;tt<4;tt++)
      #pragma unroll
      for(int dt=0;dt<5;dt++){ if(i==0) MFMA16Z(acc[tt][dt],W[i][dt],H[i][tt]); else MFMA16(acc[tt][dt],W[i][dt],H[i][tt]); }
    GAP();
  }
  asm volatile("s_nop 15":::"memory");
  GAP();
  #pragma unroll
  for(int tt=0;tt<4;tt++) if(tt!=(int)w)
    #pragma unroll
    for(int dt=0;dt<5;dt++) *(f32x4*)(smem+((w*4+tt)*5+dt)*1024+lane*16)=acc[tt][dt];
  f32x4 own[5];
  #pragma unroll
  for(int dt=0;dt<5;dt++) own[dt]= w==0? acc[0][dt] : w==1? acc[1][dt] : w==2? acc[2][dt] : acc[3][dt];
  __syncthreads();
  const float m=bflo(stv), rd=1.f/bfhi(stv);
  #pragma unroll
  for(int dt=0;dt<5;dt++){
    f32x4 f=own[dt];
    #pragma unroll
    for(int sw=0;sw<4;sw++) if(sw!=(int)w) f+=*(const f32x4*)(smem+((sw*4+w)*5+dt)*1024+lane*16);
    s16x4 yy=trread(smem+G2_Y+(16*dt+4*q+((lane&15)>>2))*160+(16*w+4*(lane&3))*2);
    u32x2 y2=__builtin_bit_cast(u32x2,yy);
    float xf[4]={bflo(xv[dt][0]),bfhi(xv[dt][0]),bflo(xv[dt][1]),bfhi(xv[dt][1])};
    float yf[4]={bflo(y2[0]),bfhi(y2[0]),bflo(y2[1]),bfhi(y2[1])};
    float o[4];
    #pragma unroll
    for(int e=0;e<4;e++){ float xn=rbf(rbf(xf[e]-m)*rd); float a=rbf(xn*yf[e]); o[e]=rbf(xf[e]+a)+rbf(f[e]); }
    u32x2 ov={cvtpk(o[0],o[1]),cvtpk(o[2],o[3])};
    *(u32x2*)(out+(size_t)trow*D_+80*nt+16*dt+4*q)=ov;
  }
}

extern "C" void kernel_launch(void* const* d_in, const int* in_sizes, int n_in, void* d_out, int out_size, void* d_ws, size_t ws_size, hipStream_t stream){
  const u16* q=(const u16*)d_in[0]; const u16* k=(const u16*)d_in[1]; const u16* v=(const u16*)d_in[2];
  const u16* sk=(const u16*)d_in[3]; const u16* h=(const u16*)d_in[4]; const u16* Dv=(const u16*)d_in[5];
  const u16* wg=(const u16*)d_in[7]; const u16* wu=(const u16*)d_in[8]; const u16* wd=(const u16*)d_in[9];
  char* ws=(char*)d_ws;
  (void)hipFuncSetAttribute((const void*)k_gemm1,hipFuncAttributeMaxDynamicSharedMemorySize,3*G1_STG);
  (void)hipFuncSetAttribute((const void*)k_conv,hipFuncAttributeMaxDynamicSharedMemorySize,4*CV_CH);
  (void)hipFuncSetAttribute((const void*)k_gemm2,hipFuncAttributeMaxDynamicSharedMemorySize,G2_Y+12800);
  k_prep<<<256,256,32768,stream>>>(q,k,v,sk,wd,ws);
  k_gemm1<<<256,256,3*G1_STG,stream>>>(wg,wu,ws);
  k_conv<<<160,256,4*CV_CH,stream>>>(h,Dv,ws);
  k_gemm2<<<256,256,G2_Y+12800,stream>>>(q,ws,(u16*)d_out);
}
```

```cpp
#include <hip/hip_runtime.h>
#include <hip/hip_bf16.h>
#include <stdint.h>
#define LAS __attribute__((address_space(3)))
#define GAS __attribute__((address_space(1)))
#define DI __device__ __forceinline__
typedef unsigned short u16;
typedef unsigned long long u64;
typedef short s16x4 __attribute__((ext_vector_type(4)));
typedef short s16x8 __attribute__((ext_vector_type(8)));
typedef __bf16 bf16x2 __attribute__((ext_vector_type(2)));
typedef float f32x2 __attribute__((ext_vector_type(2)));
typedef float f32x4 __attribute__((ext_vector_type(4)));
typedef float f32x16 __attribute__((ext_vector_type(16)));
typedef unsigned u32x4 __attribute__((ext_vector_type(4)));
typedef unsigned u32x2 __attribute__((ext_vector_type(2)));
constexpr int S_=2048, D_=640, F_=1728;
#define GAP() __builtin_amdgcn_sched_barrier(0)
DI unsigned rfl(unsigned x){ return __builtin_amdgcn_readfirstlane(x); }
DI u32x4 mk_rsrc(const void* p, unsigned bytes){
  unsigned long long a=(unsigned long long)p;
  u32x4 r; r[0]=rfl((unsigned)a); r[1]=rfl((unsigned)(a>>32))&0xffffu; r[2]=bytes; r[3]=0x00020000u; return r;
}
#define SETM0(x) asm volatile("s_mov_b32 m0, %0\n\ts_nop 0"::"s"(x))
#define DMA(voff,rsrc,soff,imm) asm volatile("buffer_load_dwordx4 %0, %1, %2 offen offset:" #imm " lds"::"v"(voff),"s"(rsrc),"s"(soff))
#define DMAS(voff,rsrc,soff,imm) asm volatile("buffer_load_dwordx4 %0, %1, %2 offen offset:" #imm " sc1 lds"::"v"(voff),"s"(rsrc),"s"(soff))
#define LD4(dst,voff,rsrc,soff,imm) asm volatile("buffer_load_dwordx4 %0, %1, %2, %3 offen offset:" #imm:"=v"(dst):"v"(voff),"s"(rsrc),"s"(soff))
#define LD4S(dst,voff,rsrc,soff,imm) asm volatile("buffer_load_dwordx4 %0, %1, %2, %3 offen offset:" #imm " sc1":"=v"(dst):"v"(voff),"s"(rsrc),"s"(soff))
#define LD4SA(dst,voff,rsrc,soff,imm) asm volatile("buffer_load_dwordx4 %0, %1, %2, %3 offen offset:" #imm " sc1":"=a"(dst):"v"(voff),"s"(rsrc),"s"(soff))
#define LD2S(dst,voff,rsrc,soff,imm) asm volatile("buffer_load_dwordx2 %0, %1, %2, %3 offen offset:" #imm " sc1":"=v"(dst):"v"(voff),"s"(rsrc),"s"(soff))
#define LD2(dst,voff,rsrc,soff,imm) asm volatile("buffer_load_dwordx2 %0, %1, %2, %3 offen offset:" #imm:"=v"(dst):"v"(voff),"s"(rsrc),"s"(soff))
#define LD1S(dst,voff,rsrc,soff,imm) asm volatile("buffer_load_dword %0, %1, %2, %3 offen offset:" #imm " sc1":"=v"(dst):"v"(voff),"s"(rsrc),"s"(soff))
#define ST4S(src,voff,rsrc,soff,imm) asm volatile("buffer_store_dwordx4 %0, %1, %2, %3 offen offset:" #imm " sc1\n\ts_nop 1"::"v"(src),"v"(voff),"s"(rsrc),"s"(soff))
#define ST2S(src,voff,rsrc,soff,imm) asm volatile("buffer_store_dwordx2 %0, %1, %2, %3 offen offset:" #imm " sc1"::"v"(src),"v"(voff),"s"(rsrc),"s"(soff))
#define LDH LD4S
#define DMAX DMAS
#define WAITV(n) asm volatile("s_waitcnt vmcnt(" #n ")":::"memory")
template<int N> DI void waitv(){ asm volatile("s_waitcnt vmcnt(%0)"::"n"(N):"memory"); }
#define BARRIER() asm volatile("s_waitcnt lgkmcnt(0)\n\ts_barrier":::"memory")
#define MFMA32(acc,a,b) asm volatile("v_mfma_f32_32x32x16_bf16 %0, %1, %2, %0":"+v"(acc):"v"(a),"v"(b))
#define MFMA32Z(acc,a,b) asm volatile("v_mfma_f32_32x32x16_bf16 %0, %1, %2, 0":"=&v"(acc):"v"(a),"v"(b))
#define MFMA16(acc,a,b) asm volatile("v_mfma_f32_16x16x32_bf16 %0, %1, %2, %0":"+v"(acc):"v"(a),"v"(b))
#define MFMA16Z(acc,a,b) asm volatile("v_mfma_f32_16x16x32_bf16 %0, %1, %2, 0":"=&v"(acc):"v"(a),"v"(b))
#define MFMA16A(acc,a,b) asm volatile("v_mfma_f32_16x16x32_bf16 %0, %1, %2, %0":"+v"(acc):"a"(a),"v"(b))
#define MFMA16AZ(acc,a,b) asm volatile("v_mfma_f32_16x16x32_bf16 %0, %1, %2, 0":"=&v"(acc):"a"(a),"v"(b))
DI float bf2f(unsigned x16){ return __uint_as_float(x16<<16); }
DI float bflo(unsigned x){ return __uint_as_float(x<<16); }
DI float bfhi(unsigned x){ return __uint_as_float(x&0xffff0000u); }
DI unsigned cvtpk(float lo,float hi){ f32x2 v={lo,hi}; bf16x2 b=__builtin_convertvector(v,bf16x2); return __builtin_bit_cast(unsigned,b); }
DI float rbf(float x){ return bflo(cvtpk(x,x)); }
DI u32x4 cat(s16x4 a, s16x4 b){ s16x8 r=__builtin_shufflevector(a,b,0,1,2,3,4,5,6,7); return __builtin_bit_cast(u32x4,r); }
DI u32x4 cat2(u32x2 a,u32x2 b){ return __builtin_shufflevector(a,b,0,1,2,3); }
DI s16x4 trread(const char* p){ return __builtin_amdgcn_ds_read_tr16_b64_v4i16((LAS s16x4*)p); }

constexpr unsigned WS_E=0, WS_FX=256, WS_FP=WS_FX+8192, WS_FH=WS_FP+2048, WS_FY=WS_FH+8192, WS_FLAGS_END=WS_FY+5120;
constexpr unsigned WS_ST=24576, WS_XN=32768, WS_UT=WS_XN+S_*D_*2, WS_WDF=WS_UT+D_*S_*2, WS_HF=WS_WDF+F_*D_*2, WS_YT=WS_HF+64*108*1024, WS_END=WS_YT+D_*S_*2;
constexpr unsigned WS_DBG=32u<<20;
static_assert(WS_FLAGS_END<=WS_ST,"");
constexpr int G1_XB=16384, G1_GB=14336, G1_STG=G1_XB+G1_GB+128+G1_GB;
constexpr int L_PREP=3*G1_STG;
constexpr int CV_HC=4160, CV_H=4*CV_HC, CV_U=4608, CV_CH=CV_H+CV_U;
constexpr int L_CV=G1_STG;
constexpr int L_Y=L_PREP;
constexpr int LDS_BYTES=163840;

#define STAMP(i)

DI void spin_flags(const GAS u64* p, bool active, u64 tag){
  const u64 t0=__builtin_amdgcn_s_memrealtime();
  for(;;){
    u64 v= active? __hip_atomic_load(p,__ATOMIC_RELAXED,__HIP_MEMORY_SCOPE_AGENT) : tag;
    if(__all(v==tag)) break;
    if(__builtin_amdgcn_s_memrealtime()-t0>2000000ull) break;
    __builtin_amdgcn_s_sleep(2);
  }
  asm volatile("s_waitcnt vmcnt(0)":::"memory");
}
DI void put_flag(char* ws, unsigned off, u64 tag){ __hip_atomic_store((GAS u64*)(ws+off),tag,__ATOMIC_RELAXED,__HIP_MEMORY_SCOPE_AGENT); }
DI float wsum32(float v){ v+=__shfl_xor(v,1); v+=__shfl_xor(v,2); v+=__shfl_xor(v,4); v+=__shfl_xor(v,8); v+=__shfl_xor(v,16); return v; }

struct G1Frag { u32x4 w[4]; u32x4 x[2]; };

__global__ __launch_bounds__(256,1) void fused_kernel(const u16* __restrict__ x,const u16* __restrict__ kk,const u16* __restrict__ vv,const u16* __restrict__ sk,
    const u16* __restrict__ hk,const u16* __restrict__ Dv,const u16* __restrict__ wg,const u16* __restrict__ wu,const u16* __restrict__ wd,u16* __restrict__ out,char* ws){
  extern __shared__ __attribute__((aligned(16))) char smem[];
  STAMP(0);
  const int tid=threadIdx.x, lane=tid&63; const unsigned w=rfl(tid>>6); const int b=blockIdx.x;
  const unsigned lds0=(unsigned)(size_t)(LAS char*)smem;
  const u32x4 rws=mk_rsrc(ws,WS_END);
  constexpr unsigned OOB=0xfff00000u;
  const u32x4 rx=mk_rsrc(x,S_*D_*2);
  u32x2 Eraw; { const unsigned z=0; LD2S(Eraw,z,rws,0,0); }
  const int lrow=8*b+2*w+(lane>>5), l32=lane&31;
  u32x4 xc3[3];
  { const unsigned vo=lrow*1280+l32*16; LD4(xc3[0],vo,rx,0,0); LD4(xc3[1],vo,rx,0,512); const unsigned v2=(vo+1024)|(l32<16?0:OOB); LD4(xc3[2],v2,rx,0,0); }
  u32x4 pr[15];
  const bool iskv=b<160;
  const int tb=b/5, cb=b-5*tb, t0=tb*64, pc=tid&15, tg=tid>>4, d0=cb*128+pc*8;
  const int gw0=4*(b-160)+w, gw1=gw0+384;
  { const u32x4 rk=mk_rsrc(iskv?kk:wd, iskv? S_*D_*2 : F_*D_*2), rv=mk_rsrc(iskv?vv:wd, iskv? S_*D_*2 : F_*D_*2), rsk=mk_rsrc(sk,D_*8);
    const int s0=gw0/10, c0=gw0-10*s0, s1=gw1/10, c1=gw1-10*s1;
    const unsigned wo0=((32*s0+(lane>>3))*D_+c0*64+(lane&7)*8)*2, wo1=((32*s1+(lane>>3))*D_+c1*64+(lane&7)*8)*2, m1=gw1<540?0:OOB;
    #pragma unroll
    for(int i=0;i<7;i++){ const int t=t0+4*tg-3+i; const unsigned kv_o=((t*D_+d0)*2)|(t<0?OOB:0);
      const unsigned wd_o= i<4? wo0+i*8*1280 : (wo1+(i-4)*8*1280)|m1;
      const unsigned vo= iskv? kv_o: wd_o; LD4(pr[i],vo,rk,0,0); }
    #pragma unroll
    for(int i=0;i<4;i++){ const unsigned kv_o=((t0+4*tg+i)*D_+d0)*2; const unsigned wd_o= i==0? (wo1+3*8*1280)|m1 : OOB;
      const unsigned vo= iskv? kv_o: wd_o; LD4(pr[7+i],vo,rv,0,0); }
    #pragma unroll
    for(int i=0;i<4;i++){ const unsigned vo=(d0*8+i*16)|(iskv?0:OOB); LD4(pr[11+i],vo,rsk,0,0); }
  }
  asm volatile("s_waitcnt vmcnt(15)":"+v"(Eraw),"+v"(xc3[0]),"+v"(xc3[1]),"+v"(xc3[2])::"memory");
  const u64 tag=((u64)rfl(Eraw[0])|((u64)rfl(Eraw[1])<<32))*6364136223846793005ull+1442695040888963407ull;
  {
    float f[24];
    #pragma unroll
    for(int i=0;i<3;i++) for(int e=0;e<4;e++){ f[8*i+2*e]=bflo(xc3[i][e]); f[8*i+2*e+1]=bfhi(xc3[i][e]); }
    float s=0;
    #pragma unroll
    for(int i=0;i<24;i++) s+=f[i];
    const float mean=wsum32(s)*(1.f/640.f);
    float q=0;
    #pragma unroll
    for(int i=0;i<16;i++){ float d=f[i]-mean; q=fmaf(d,d,q); }
    float q2=0;
    #pragma unroll
    for(int i=16;i<24;i++){ float d=f[i]-mean; q2=fmaf(d,d,q2); }
    q+= l32<16? q2:0.f;
    const float var=rbf(wsum32(q)*(1.f/640.f)); const float m=rbf(mean);
    const float den=rbf(sqrtf(rbf(var+1e-5f))); const float rd=1.f/den;
    #pragma unroll
    for(int i=0;i<3;i++){ u32x4 o; for(int e=0;e<4;e++) o[e]=cvtpk(rbf(f[8*i+2*e]-m)*rd, rbf(f[8*i+2*e+1]-m)*rd);
      const unsigned vo=(WS_XN+lrow*1280+(l32+32*i)*16)|((i==2 && l32>=16)?OOB:0);
      ST4S(o,vo,rws,0,0); }
    unsigned sv=cvtpk(m,den); const unsigned vo=(WS_ST+lrow*4)|(l32==0?0:OOB);
    asm volatile("buffer_store_dword %0, %1, %2, 0 offen sc1"::"v"(sv),"v"(vo),"s"(rws));
  }
  STAMP(1);
  #define PRREGS "+v"(pr[0]),"+v"(pr[1]),"+v"(pr[2]),"+v"(pr[3]),"+v"(pr[4]),"+v"(pr[5]),"+v"(pr[6]),"+v"(pr[7]),"+v"(pr[8]),"+v"(pr[9]),"+v"(pr[10]),"+v"(pr[11]),"+v"(pr[12]),"+v"(pr[13]),"+v"(pr[14])
  asm volatile("s_waitcnt vmcnt(4)":PRREGS::"memory");
  char* P=smem+L_PREP;
  u32x4 po[8];
  if(iskv){
    #pragma unroll
    for(int e=0;e<8;e++){
      const unsigned a=pr[11+(e>>1)][(e&1)*2], bb=pr[11+(e>>1)][(e&1)*2+1];
      const float w0=bflo(a),w1=bfhi(a),w2=bflo(bb),w3=bfhi(bb);
      float kf[7];
      #pragma unroll
      for(int i=0;i<7;i++) kf[i]=(e&1)? bfhi(pr[i][e>>1]) : bflo(pr[i][e>>1]);
      float o[4];
      #pragma unroll
      for(int i=0;i<4;i++){ float ks=kf[i]*w3; ks=fmaf(kf[i+1],w2,ks); ks=fmaf(kf[i+2],w1,ks); ks=fmaf(kf[i+3],w0,ks);
        const float vf=(e&1)? bfhi(pr[7+i][e>>1]) : bflo(pr[7+i][e>>1]); o[i]=ks*vf; }
      u32x2 pk={cvtpk(o[3],o[2]),cvtpk(o[1],o[0])};
      *(u32x2*)(P+(8*pc+e)*128+((15-tg)^pc)*8)=pk;
    }
  } else {
    char* L=P+w*4096;
    const int g=lane>>4, q=(lane&15)>>2, p=lane&3;
    #pragma unroll
    for(int z=0;z<2;z++){
      #pragma unroll
      for(int i=0;i<4;i++){ const int r=8*i+(lane>>3); *(u32x4*)(L+r*128+(((lane&7)^((r>>2)&7))*16))=pr[4*z+i]; }
      asm volatile("s_waitcnt lgkmcnt(0)":::"memory");
      #pragma unroll
      for(int f4=0;f4<4;f4++){
        s16x4 h[2];
        #pragma unroll
        for(int t=0;t<2;t++){ const int r=8*g+4*t+q; const int ch=2*f4+(p>>1); h[t]=trread(L+r*128+((ch^((r>>2)&7))*16)+(p&1)*8); }
        po[4*z+f4]=cat(h[0],h[1]);
      }
      asm volatile("s_waitcnt lgkmcnt(0)":::"memory");
    }
  }
  const int xcd=b&7, jj=b>>3; const int mi=(xcd>>2)*8+(jj&7), ni=(xcd&3)*4+(jj>>3);
  const u32x4 rw=mk_rsrc(w<2?wg:wu, D_*F_*2);
  unsigned vx[4], vw[7];
  #pragma unroll
  for(int i=0;i<4;i++){ int row=32*w+8*i+(lane>>3); int c=(lane&7)^((row>>1)&7); vx[i]=WS_XN+(128*mi+row)*1280+c*16-1024*i; }
  #pragma unroll
  for(int i=0;i<7;i++){ int q=(w&1)*7+i; int c=64*q+lane; int k=c/14, cc=c-14*k; vw[i]=k*3456+ni*224+cc*16-1024*(i&3); }
  const unsigned m0x=rfl(lds0+w*4096), m0w=rfl(lds0+G1_XB+(w>>1)*(G1_GB+128)+(w&1)*7168);
  auto issue_x=[&](int s,int buf){ unsigned so=s*128; SETM0(m0x+buf*G1_STG); DMAX(vx[0],rws,so,0); DMAX(vx[1],rws,so,1024); DMAX(vx[2],rws,so,2048); DMAX(vx[3],rws,so,3072); };
  auto issue_w0=[&](int s,int buf){ unsigned so=s*221184; SETM0(m0w+buf*G1_STG); DMA(vw[0],rw,so,0); DMA(vw[1],rw,so,1024); DMA(vw[2],rw,so,2048); DMA(vw[3],rw,so,3072); };
  auto issue_w1=[&](int s,int buf){ unsigned so=s*221184; SETM0(m0w+buf*G1_STG+4096); DMA(vw[4],rw,so,0); DMA(vw[5],rw,so,1024); DMA(vw[6],rw,so,2048); };
  WAITV(0);
  if(lane==0) put_flag(ws,WS_FX+(4*b+w)*8,tag);
  STAMP(2);
  issue_w0(0,0); issue_w1(0,0); issue_w0(1,1); issue_w1(1,1); issue_w0(2,2); issue_w1(2,2);
  BARRIER();
  if(iskv){
    #pragma unroll
    for(int i=0;i<4;i++){ const int id=tid+256*i, r=id>>3, ch=id&7, cc=r>>3;
      u32x4 v=*(const u32x4*)(P+r*128+((ch^(cc>>1))*16));
      if(cc&1) v=u32x4{v[2],v[3],v[0],v[1]};
      const unsigned vo=WS_UT+(cb*128+r)*4096+(S_-64-t0+ch*8)*2; ST4S(v,vo,rws,0,0); }
  } else {
    #pragma unroll
    for(int z=0;z<2;z++){ const int gw= z? gw1: gw0; const int s=gw/10, c64=gw-10*s;
      #pragma unroll
      for(int f4=0;f4<4;f4++){ const int D16=c64*4+f4, nt=D16/5, dt=D16-5*nt;
        const unsigned vo=(WS_WDF+(((nt*54+s)*5+dt)*64+lane)*16)|(gw<540?0:OOB); ST4S(po[4*z+f4],vo,rws,0,0); } }
  }
  STAMP(3);
  spin_flags((const GAS u64*)(ws+WS_FX)+64*mi+lane,true,tag);
  STAMP(4);
  issue_x(0,0); issue_x(1,1); issue_x(2,2);
  const int wm=w>>1, wn=w&1;
  int xa[4];
  #pragma unroll
  for(int j=0;j<4;j++) xa[j]=(64*wm+32*wn+(lane&31))*128+(((2*j+(lane>>5))^((lane>>1)&7))*16);
  const int xo=wn? -4096:4096;
  const int pperm=((lane&1)<<1|((lane>>1)&1));
  const int wa=G1_XB+((lane>>4)&1)*(G1_GB+128)+(8*(lane>>5)+((lane&15)>>2))*224+pperm*8;
  const int wt0=wn*4;
  f32x16 acc[7];
  auto ldw=[&](int buf,int j,int tile)->u32x4{ const char* p=smem+buf*G1_STG+wa+(16*j)*224+tile*32; return cat(trread(p),trread(p+4*224)); };
  auto ldx=[&](int buf,int j,int ms)->u32x4{ return *(const u32x4*)(smem+buf*G1_STG+xa[j]+(ms? xo:0)); };
  const int cch= b<160? 4*b+w : 4*(b-160)+w;
  const u32x4 rhk=mk_rsrc(hk,D_*S_*2);
  const unsigned ldsC=rfl(lds0+L_CV+w*CV_CH); char* LC=smem+L_CV+w*CV_CH;
  auto issue_hc=[&](int c){ unsigned vo=lane*16+2*c; unsigned so=cch*4096; SETM0(ldsC+c*CV_HC+32); DMA(vo,rhk,so,0); DMA(vo,rhk,so,1024); DMA(vo,rhk,so,2048); DMA(vo,rhk,so,3072); };
  u32x4 fp0,fp1;
  G1Frag fr[2];
  WAITV(8); BARRIER();
  if(tid==0) put_flag(ws,WS_FP+b*8,tag);
  STAMP(5);
  fr[0].x[0]=ldx(0,0,0); fr[0].x[1]=ldx(0,0,1);
  #pragma unroll
  for(int t=0;t<3;t++) fr[0].w[t]=ldw(0,0,wt0+t);
  fr[0].w[3]=ldw(0,0,3);
  GAP();
  #pragma unroll
  for(int s=0;s<10;s++){
    const int buf=s%3;
    #pragma unroll
    for(int j=0;j<4;j++){
      G1Frag& c=fr[j&1]; G1Frag& n=fr[(j&1)^1];
      const bool has_next = !(s==9 && j==3);
      const int nb = j==3 ? (s+1)%3 : buf, nj=(j+1)&3;
      if(j==3 && s<9){ if(s==0) WAITV(4); else if(s<=7) WAITV(11); else WAITV(0); BARRIER(); }
      #define MF(t,wi,xi) do{ if(s==0&&j==0) MFMA32Z(acc[t],c.w[wi],c.x[xi]); else MFMA32(acc[t],c.w[wi],c.x[xi]); }while(0)
      MF(0,0,0); if(has_next){ n.x[0]=ldx(nb,nj,0); n.w[0]=ldw(nb,nj,wt0+0);} GAP();
      MF(3,0,1); if(has_next){ n.w[1]=ldw(nb,nj,wt0+1);} GAP();
      MF(1,1,0); if(has_next){ n.x[1]=ldx(nb,nj,1); n.w[2]=ldw(nb,nj,wt0+2);} GAP();
      MF(4,1,1); if(has_next){ n.w[3]=ldw(nb,nj,3);} GAP();
      MF(2,2,0);
      if(j==3 && s+3<10) issue_x(s+3,buf);
      if(j==0 && s>=1 && s+2<10) issue_w0(s+2,(s+2)%3);
      if(j==1 && s>=1 && s+2<10) issue_w1(s+2,(s+2)%3);
      if(s==8 && j==3){ const unsigned vo=WS_FP+lane*32; LD4S(fp0,vo,rws,0,0); LD4S(fp1,vo,rws,0,16); issue_hc(0); }
      if(s==9 && j<3) issue_hc(j+1);
      GAP();
      MF(5,2,1); GAP();
      MF(6,3,0); GAP();
    }
  }
  STAMP(6);
  asm volatile("s_waitcnt vmcnt(16)":"+v"(fp0),"+v"(fp1)::"memory");
  { const unsigned tl=(unsigned)tag, th=(unsigned)(tag>>32);
    const bool ok= fp0[0]==tl&&fp0[1]==th&&fp0[2]==tl&&fp0[3]==th&&fp1[0]==tl&&fp1[1]==th&&fp1[2]==tl&&fp1[3]==th;
    if(!__all(ok)){
      #pragma unroll 1
      for(int i=0;i<4;i++) spin_flags((const GAS u64*)(ws+WS_FP)+64*i+lane,true,tag);
    }
    if(b==0 && tid==0) put_flag(ws,WS_E,tag);
  }
  u32x4 ur[4]; unsigned hh, dvr;
  { const unsigned vo=WS_UT+cch*4096+lane*16; LD4S(ur[0],vo,rws,0,0); LD4S(ur[1],vo,rws,0,1024); LD4S(ur[2],vo,rws,0,2048); LD4S(ur[3],vo,rws,0,3072);
    const int c=lane>>4,p=lane&15; const unsigned vh=((cch*S_+p-16+c)*2)|(p>=16-c?0:OOB);
    asm volatile("buffer_load_ushort %0, %1, %2, 0 offen":"=v"(hh):"v"(vh),"s"(rhk));
    const u32x4 rdv=mk_rsrc(Dv,D_*2); const unsigned vd=cch*2;
    asm volatile("buffer_load_ushort %0, %1, %2, 0 offen":"=v"(dvr):"v"(vd),"s"(rdv)); }
  asm volatile("s_nop 15":"+v"(acc[0]),"+v"(acc[1]),"+v"(acc[2]),"+v"(acc[3]),"+v"(acc[4]),"+v"(acc[5]),"+v"(acc[6]));
  { const float L2E=1.4426950408889634f;
    #pragma unroll
    for(int t=0;t<7;t++){
      const int tile = t==6?3: wt0+(t%3); const int ms = (t==6||t<3)? wn : 1-wn;
      const int ft=ni*7+tile;
      u32x4 o;
      #pragma unroll
      for(int e=0;e<4;e++){
        f32x2 g={acc[t][2*e],acc[t][2*e+1]}, u={acc[t][8+2*e],acc[t][8+2*e+1]};
        f32x2 s=g*(-L2E); f32x2 ex={__builtin_amdgcn_exp2f(s[0]),__builtin_amdgcn_exp2f(s[1])};
        f32x2 d=ex+1.f; f32x2 rc={__builtin_amdgcn_rcpf(d[0]),__builtin_amdgcn_rcpf(d[1])};
        f32x2 h=(g*u)*rc; o[e]=cvtpk(h[0],h[1]);
      }
      const int mb=mi*4+wm*2+ms;
      const unsigned vo=(WS_HF+((mb*108+ft)*64+lane)*16)|(ft<108?0:OOB);
      ST4S(o,vo,rws,0,0);
    }
  }
  STAMP(7);
  asm volatile("s_waitcnt vmcnt(7)":"+v"(ur[0]),"+v"(ur[1]),"+v"(ur[2]),"+v"(ur[3]),"+v"(hh),"+v"(dvr)::"memory");
  const float Dd=bf2f(dvr);
  #pragma unroll
  for(int i=0;i<4;i++) *(u32x4*)(LC+CV_H+(lane+64*i)*16)=ur[i];
  if(lane<32) *(u32x4*)(LC+CV_H+4096+lane*16)=u32x4{0,0,0,0};
  *(u16*)(LC+(lane>>4)*CV_HC+(lane&15)*2)=(u16)hh;
  asm volatile("s_waitcnt lgkmcnt(0)":::"memory");
  const int mt=(xcd>>1)*8+(jj&7), nt=(xcd&1)*4+(jj>>3);
  const unsigned vw0=lane*16;
  u32x4 WA[60];
  u32x2 ph;
  const int ci=lane&15,cq=lane>>4,cc=(ci+1)&3;
  const char* pa=LC+cc*CV_HC+2*(1+ci-cc+8*cq);
  const char* pb=LC+CV_H+2*(2032-16*ci+8*cq);
  f32x4 ya[8];
  {
    u32x4 B[2][8]; u32x4 A[2];
    auto ldA=[&](int rho,int a)->u32x4{ const char* p=pa+64*rho+512*a; return cat2(*(const u32x2*)p,*(const u32x2*)(p+8)); };
    auto ldB=[&](int rho,int bb)->u32x4{ return *(const u32x4*)(pb+64*rho-512*bb); };
    #pragma unroll
    for(int bb=0;bb<8;bb++) B[0][bb]=ldB(0,bb);
    A[0]=ldA(0,7);
    GAP();
    #pragma unroll
    for(int rho=0;rho<8;rho++){
      if(rho==1){ WAITV(8); if(lane==0) put_flag(ws,WS_FH+(mi*64+ni*4+w)*8,tag); }
      if(rho==6){ const unsigned vo=WS_FH+((mt>>1)*64+lane)*8; LD2S(ph,vo,rws,0,0); }
      #pragma unroll
      for(int aa=0;aa<8;aa++){ const int a=7-aa; const int cur=(rho*8+aa)&1;
        if(aa<7) A[cur^1]=ldA(rho,a-1); else if(rho<7) A[cur^1]=ldA(rho+1,7);
        if(rho<7) B[(rho&1)^1][aa]=ldB(rho+1,aa);
        { const int n=rho*8+aa; if(n<60){ const int i=n/5, dt=n-5*i; const int s=14*w+i;
            const unsigned so=WS_WDF+((nt*54+s)*5+dt)*1024; LD4SA(WA[n],vw0,rws,so,0); } }
        #pragma unroll
        for(int nu=a;nu<8;nu++){
          if(rho==0 && nu==a) MFMA16Z(ya[nu],A[cur],B[rho&1][nu-a]); else MFMA16(ya[nu],A[cur],B[rho&1][nu-a]);
        }
        GAP();
      }
    }
  }
  STAMP(8);
  asm volatile("s_waitcnt vmcnt(12)":"+v"(ph)::"memory");
  if(!__all(ph[0]==(unsigned)tag && ph[1]==(unsigned)(tag>>32))) spin_flags((const GAS u64*)(ws+WS_FH)+(mt>>1)*64+lane,true,tag);
  const int gj=lane&15,gq=lane>>4;
  const unsigned vh0=WS_HF+((gq>>1)*64+(gq&1)*32+gj)*16, vh1=vh0+108*1024;
  u32x4 H[14][4], WV[2][5];
  auto ldH=[&](int i){ const int s=14*w+i;
    const unsigned sh= s<54? (unsigned)((2*mt*108+2*s)*1024) : 0x10000000u;

    LDH(H[i][0],vh0,rws,sh,0); LDH(H[i][1],vh0,rws,sh,256); LDH(H[i][2],vh1,rws,sh,0); LDH(H[i][3],vh1,rws,sh,256); };
  auto ldWV=[&](int z){ const int s=14*w+12+z; const unsigned sw= s<54? (unsigned)(WS_WDF+((nt*54+s)*5)*1024) : 0x10000000u; const unsigned vw1=vw0+4096;
    LD4S(WV[z][0],vw0,rws,sw,0); LD4S(WV[z][1],vw0,rws,sw,1024); LD4S(WV[z][2],vw0,rws,sw,2048); LD4S(WV[z][3],vw0,rws,sw,3072); LD4S(WV[z][4],vw1,rws,sw,0); };
  ldH(0); ldH(1); ldH(2); ldH(3);
  asm volatile("s_nop 7":"+v"(ya[0]),"+v"(ya[1]),"+v"(ya[2]),"+v"(ya[3]),"+v"(ya[4]),"+v"(ya[5]),"+v"(ya[6]),"+v"(ya[7]));
  { unsigned pk[8][2];
    #pragma unroll
    for(int nu=0;nu<8;nu++){ const int t=256*nu+16*ci+4*cq;
      u32x2 uu=*(const u32x2*)(LC+CV_H+2*(2044-t));
      float y0=fmaf(bfhi(uu[1]),Dd,ya[nu][0]), y1=fmaf(bflo(uu[1]),Dd,ya[nu][1]), y2=fmaf(bfhi(uu[0]),Dd,ya[nu][2]), y3=fmaf(bflo(uu[0]),Dd,ya[nu][3]);
      pk[nu][0]=cvtpk(y0,y1); pk[nu][1]=cvtpk(y2,y3); }
    #pragma unroll
    for(int nu=0;nu<8;nu+=2){
      auto r0=__builtin_amdgcn_permlane16_swap(pk[nu][0],pk[nu+1][0],false,false);
      auto r1=__builtin_amdgcn_permlane16_swap(pk[nu][1],pk[nu+1][1],false,false);
      u32x4 v={r0[0],r1[0],r0[1],r1[1]};
      const int t=256*(nu+(cq&1))+16*ci+8*(cq>>1);
      const unsigned vo=(WS_YT+cch*4096+t*2)|(b<160?0:OOB);
      ST4S(v,vo,rws,0,0);
    }
  }
  STAMP(9);
  const int trow=64*mt+16*w+gj;
  u32x2 xv[5]; unsigned stv; u32x4 py; u32x4 yv[3];
  f32x4 oa[4][5];
  #define HREGS(i) "+v"(H[i][0]),"+v"(H[i][1]),"+v"(H[i][2]),"+v"(H[i][3])
  #pragma unroll
  for(int i=0;i<14;i++){
    if(i+4<14) ldH(i+4);
    if(i==8) ldWV(0);
    if(i==9) ldWV(1);
    if(i==5){
      const unsigned vo= lane<40? WS_FY+(80*nt+2*lane)*8 : WS_FY+(80*nt)*8; LD4S(py,vo,rws,0,0); }
    if(i==6){ const unsigned vo=(trow*D_+80*nt+4*gq)*2;
      LD2(xv[0],vo,rx,0,0); LD2(xv[1],vo,rx,0,32); LD2(xv[2],vo,rx,0,64); LD2(xv[3],vo,rx,0,96); LD2(xv[4],vo,rx,0,128);
      const unsigned vs=WS_ST+trow*4; LD1S(stv,vs,rws,0,0); }
    if(i==0) asm volatile("s_waitcnt vmcnt(20)":HREGS(0)::"memory");
    if(i==1) asm volatile("s_waitcnt vmcnt(20)":HREGS(1)::"memory");
    if(i==2) asm volatile("s_waitcnt vmcnt(20)":HREGS(2)::"memory");
    if(i==3) asm volatile("s_waitcnt vmcnt(20)":HREGS(3)::"memory");
    if(i==4){ asm volatile("s_waitcnt vmcnt(16)":HREGS(4)::"memory");
              if(lane==0 && b<160) put_flag(ws,WS_FY+cch*8,tag); }
    if(i==5) asm volatile("s_waitcnt vmcnt(17)":HREGS(5)::"memory");
    if(i==6) asm volatile("s_waitcnt vmcnt(23)":HREGS(6)::"memory");
    if(i==7) asm volatile("s_waitcnt vmcnt(23)":HREGS(7)::"memory");
    if(i==8) asm volatile("s_waitcnt vmcnt(28)":HREGS(8)::"memory");
    if(i==9){ asm volatile("s_waitcnt vmcnt(32)":HREGS(9)::"memory");
              asm volatile("s_waitcnt vmcnt(31)":"+v"(py)::"memory");
              const unsigned tl=(unsigned)tag, th=(unsigned)(tag>>32);
              if(!__all(py[0]==tl&&py[1]==th&&py[2]==tl&&py[3]==th)){
                spin_flags((const GAS u64*)(ws+WS_FY)+80*nt+lane,true,tag);
                spin_flags((const GAS u64*)(ws+WS_FY)+80*nt+64+lane,lane<16,tag); }
              #pragma unroll
              for(int z=0;z<3;z++){ const int id=tid+256*z; const int r=id>>3,ch=id&7;
                const unsigned vo=(WS_YT+(80*nt+r)*4096+(64*mt+ch*8)*2)|(id<640?0:OOB); LD4S(yv[z],vo,rws,0,0); } }
    if(i==10) asm volatile("s_waitcnt vmcnt(25)":HREGS(10)::"memory");
    if(i==11) asm volatile("s_waitcnt vmcnt(21)":HREGS(11)::"memory");
    if(i==12) asm volatile("s_waitcnt vmcnt(12)":HREGS(12),"+v"(WV[0][0]),"+v"(WV[0][1]),"+v"(WV[0][2]),"+v"(WV[0][3]),"+v"(WV[0][4])::"memory");
    if(i==13) asm volatile("s_waitcnt vmcnt(3)":HREGS(13),"+v"(WV[1][0]),"+v"(WV[1][1]),"+v"(WV[1][2]),"+v"(WV[1][3]),"+v"(WV[1][4])::"memory");
    GAP();
    #pragma unroll
    for(int tt=0;tt<4;tt++)
      #pragma unroll
      for(int dt=0;dt<5;dt++){
        if(i==0) MFMA16AZ(oa[tt][dt],WA[dt],H[0][tt]);
        else if(i<12) MFMA16A(oa[tt][dt],WA[5*i+dt],H[i][tt]);
        else MFMA16(oa[tt][dt],WV[i-12][dt],H[i][tt]);
      }
    GAP();
  }
  STAMP(10);
  asm volatile("s_waitcnt vmcnt(0)":"+v"(yv[0]),"+v"(yv[1]),"+v"(yv[2]),"+v"(xv[0]),"+v"(xv[1]),"+v"(xv[2]),"+v"(xv[3]),"+v"(xv[4]),"+v"(stv)::"memory");
  BARRIER();
  #pragma unroll
  for(int z=0;z<3;z++){ const int id=tid+256*z; if(id<640) *(u32x4*)(smem+L_Y+(id>>3)*160+(id&7)*16)=yv[z]; }
  asm volatile("s_nop 7":::"memory"); GAP();
  #pragma unroll
  for(int tt=0;tt<4;tt++) if(tt!=(int)w)
    #pragma unroll
    for(int dt=0;dt<5;dt++) *(f32x4*)(smem+((w*4+tt)*5+dt)*1024+lane*16)=oa[tt][dt];
  f32x4 own[5];
  #pragma unroll
  for(int dt=0;dt<5;dt++) own[dt]= w==0? oa[0][dt] : w==1? oa[1][dt] : w==2? oa[2][dt] : oa[3][dt];
  BARRIER();
  const float m=bflo(stv), rd=1.f/bfhi(stv);
  #pragma unroll
  for(int dt=0;dt<5;dt++){
    f32x4 f=own[dt];
    #pragma unroll
    for(int sw=0;sw<4;sw++) if(sw!=(int)w) f+=*(const f32x4*)(smem+((sw*4+w)*5+dt)*1024+lane*16);
    s16x4 yy=trread(smem+L_Y+(16*dt+4*gq+((lane&15)>>2))*160+(16*w+4*(lane&3))*2);
    u32x2 y2=__builtin_bit_cast(u32x2,yy);
    float xf[4]={bflo(xv[dt][0]),bfhi(xv[dt][0]),bflo(xv[dt][1]),bfhi(xv[dt][1])};
    float yf[4]={bflo(y2[0]),bfhi(y2[0]),bflo(y2[1]),bfhi(y2[1])};
    float o[4];
    #pragma unroll
    for(int e=0;e<4;e++){ float xn=rbf(rbf(xf[e]-m)*rd); float a=rbf(xn*yf[e]); o[e]=rbf(xf[e]+a)+rbf(f[e]); }
    u32x2 ov={cvtpk(o[0],o[1]),cvtpk(o[2],o[3])};
    *(u32x2*)(out+(size_t)trow*D_+80*nt+16*dt+4*gq)=ov;
  }
  STAMP(11);
}

extern "C" void kernel_launch(void* const* d_in, const int* in_sizes, int n_in, void* d_out, int out_size, void* d_ws, size_t ws_size, hipStream_t stream){
  (void)hipFuncSetAttribute((const void*)fused_kernel,hipFuncAttributeMaxDynamicSharedMemorySize,LDS_BYTES);
  fused_kernel<<<256,256,LDS_BYTES,stream>>>((const u16*)d_in[0],(const u16*)d_in[1],(const u16*)d_in[2],(const u16*)d_in[3],(const u16*)d_in[4],(const u16*)d_in[5],
      (const u16*)d_in[7],(const u16*)d_in[8],(const u16*)d_in[9],(u16*)d_out,(char*)d_ws);
}
```

```cpp
#include <hip/hip_runtime.h>
#include <hip/hip_bf16.h>
#include <stdint.h>
#include <type_traits>
#include <utility>
#define LAS __attribute__((address_space(3)))
#define GAS __attribute__((address_space(1)))
#define DI __device__ __forceinline__
typedef unsigned short u16;
typedef unsigned long long u64;
typedef short s16x4 __attribute__((ext_vector_type(4)));
typedef short s16x8 __attribute__((ext_vector_type(8)));
typedef __bf16 bf16x2 __attribute__((ext_vector_type(2)));
typedef float f32x2 __attribute__((ext_vector_type(2)));
typedef float f32x4 __attribute__((ext_vector_type(4)));
typedef unsigned u32x4 __attribute__((ext_vector_type(4)));
typedef unsigned u32x2 __attribute__((ext_vector_type(2)));
constexpr int S_=2048, D_=640, F_=1728;
#define ROT 0
#define NTX 0
#define NTSX 0
#define NTSW 0
#define NTCS 0
#define TKM 0
#define PORD 0
#define GAP() __builtin_amdgcn_sched_barrier(0)
DI unsigned rfl(unsigned x){ return __builtin_amdgcn_readfirstlane(x); }
DI u32x4 mk_rsrc(const void* p, unsigned bytes){
  unsigned long long a=(unsigned long long)p;
  u32x4 r; r[0]=rfl((unsigned)a); r[1]=rfl((unsigned)(a>>32))&0xffffu; r[2]=bytes; r[3]=0x00020000u; return r;
}
DI void setm0(unsigned x){ asm volatile("s_mov_b32 m0, %0\n\ts_nop 0"::"s"(x)); }
template<int I> DI void dma (unsigned vo,const u32x4& r,unsigned so){ asm volatile("buffer_load_dwordx4 %0, %1, %2 offen offset:%3 lds"::"v"(vo),"s"(r),"s"(so),"n"(I)); }
template<int I> DI void dmas(unsigned vo,const u32x4& r,unsigned so){ asm volatile("buffer_load_dwordx4 %0, %1, %2 offen offset:%3 sc1 lds"::"v"(vo),"s"(r),"s"(so),"n"(I)); }
template<int I> DI void ld4  (u32x4& d,unsigned vo,const u32x4& r,unsigned so){ asm volatile("buffer_load_dwordx4 %0, %1, %2, %3 offen offset:%4":"=v"(d):"v"(vo),"s"(r),"s"(so),"n"(I)); }
template<int I> DI void dman(unsigned vo,const u32x4& r,unsigned so){ asm volatile("buffer_load_dwordx4 %0, %1, %2 offen offset:%3 nt lds"::"v"(vo),"s"(r),"s"(so),"n"(I)); }
template<int I> DI void ld4n (u32x4& d,unsigned vo,const u32x4& r,unsigned so){ asm volatile("buffer_load_dwordx4 %0, %1, %2, %3 offen offset:%4 nt":"=v"(d):"v"(vo),"s"(r),"s"(so),"n"(I)); }
template<int N,int I> DI void ldwa(unsigned vo,const u32x4& r,unsigned so){ asm volatile("buffer_load_dwordx4 a[%c3:%c4], %0, %1, %2 offen offset:%5 sc1"::"v"(vo),"s"(r),"s"(so),"n"(4*N),"n"(4*N+3),"n"(I)); }
template<int N> DI void mfmaw (f32x4& acc,const u32x4& b){ asm volatile("v_mfma_f32_16x16x32_bf16 %0, a[%c2:%c3], %1, %0":"+v"(acc):"v"(b),"n"(4*N),"n"(4*N+3)); }
template<int N> DI void mfmawz(f32x4& acc,const u32x4& b){ asm volatile("v_mfma_f32_16x16x32_bf16 %0, a[%c2:%c3], %1, 0":"=&v"(acc):"v"(b),"n"(4*N),"n"(4*N+3)); }
template<int NT,int I> DI void dmap(unsigned vo,const u32x4& r,unsigned so){ if constexpr(NT) dman<I>(vo,r,so); else dma<I>(vo,r,so); }
template<int NT,int I> DI void ld4p(u32x4& d,unsigned vo,const u32x4& r,unsigned so){ if constexpr(NT) ld4n<I>(d,vo,r,so); else ld4<I>(d,vo,r,so); }
template<int I> DI void ld1n (unsigned& d,unsigned vo,const u32x4& r,unsigned so){ asm volatile("buffer_load_dword %0, %1, %2, %3 offen offset:%4 nt":"=v"(d):"v"(vo),"s"(r),"s"(so),"n"(I)); }
template<int I> DI void ld4s (u32x4& d,unsigned vo,const u32x4& r,unsigned so){ asm volatile("buffer_load_dwordx4 %0, %1, %2, %3 offen offset:%4 sc1":"=v"(d):"v"(vo),"s"(r),"s"(so),"n"(I)); }
template<int I> DI void ld4sa(u32x4& d,unsigned vo,const u32x4& r,unsigned so){ asm volatile("buffer_load_dwordx4 %0, %1, %2, %3 offen offset:%4 sc1":"=a"(d):"v"(vo),"s"(r),"s"(so),"n"(I)); }
template<int I> DI void ld2s (u32x2& d,unsigned vo,const u32x4& r,unsigned so){ asm volatile("buffer_load_dwordx2 %0, %1, %2, %3 offen offset:%4 sc1":"=v"(d):"v"(vo),"s"(r),"s"(so),"n"(I)); }
template<int I> DI void ld2  (u32x2& d,unsigned vo,const u32x4& r,unsigned so){ asm volatile("buffer_load_dwordx2 %0, %1, %2, %3 offen offset:%4":"=v"(d):"v"(vo),"s"(r),"s"(so),"n"(I)); }
template<int I> DI void ld1s (unsigned& d,unsigned vo,const u32x4& r,unsigned so){ asm volatile("buffer_load_dword %0, %1, %2, %3 offen offset:%4 sc1":"=v"(d):"v"(vo),"s"(r),"s"(so),"n"(I)); }
template<int I> DI void ld1  (unsigned& d,unsigned vo,const u32x4& r,unsigned so){ asm volatile("buffer_load_dword %0, %1, %2, %3 offen offset:%4":"=v"(d):"v"(vo),"s"(r),"s"(so),"n"(I)); }
template<int I> DI void st4s (const u32x4& d,unsigned vo,const u32x4& r,unsigned so){ asm volatile("buffer_store_dwordx4 %0, %1, %2, %3 offen offset:%4 sc1\n\ts_nop 1"::"v"(d),"v"(vo),"s"(r),"s"(so),"n"(I)); }
template<int I> DI void st2s (const u32x2& d,unsigned vo,const u32x4& r,unsigned so){ asm volatile("buffer_store_dwordx2 %0, %1, %2, %3 offen offset:%4 sc1\n\ts_nop 1"::"v"(d),"v"(vo),"s"(r),"s"(so),"n"(I)); }
#define SETM0(x) setm0(x)
#define DMA(voff,rsrc,soff,imm) dma<imm>(voff,rsrc,soff)
#define DMAS(voff,rsrc,soff,imm) dmas<imm>(voff,rsrc,soff)
#define LD4(dst,voff,rsrc,soff,imm) ld4<imm>(dst,voff,rsrc,soff)
#define LD4S(dst,voff,rsrc,soff,imm) ld4s<imm>(dst,voff,rsrc,soff)
#define LD4SA(dst,voff,rsrc,soff,imm) ld4sa<imm>(dst,voff,rsrc,soff)
#define LD2S(dst,voff,rsrc,soff,imm) ld2s<imm>(dst,voff,rsrc,soff)
#define LD2(dst,voff,rsrc,soff,imm) ld2<imm>(dst,voff,rsrc,soff)
#define LD1S(dst,voff,rsrc,soff,imm) ld1s<imm>(dst,voff,rsrc,soff)
#define LD1(dst,voff,rsrc,soff,imm) ld1<imm>(dst,voff,rsrc,soff)
#define ST4S(src,voff,rsrc,soff,imm) st4s<imm>(src,voff,rsrc,soff)
#define ST2S(src,voff,rsrc,soff,imm) st2s<imm>(src,voff,rsrc,soff)
template<int N> DI void waitv(){ asm volatile("s_waitcnt vmcnt(%0)"::"n"(N):"memory"); }
template<class T> DI void pin(T& a){ asm volatile("":"+v"(a)::"memory"); }
template<class T,class... R> DI void pin(T& a,R&... r){ pin(a); pin(r...); }
template<int N,class... R> DI void waitv(R&... r){ asm volatile("s_waitcnt vmcnt(%0)"::"n"(N):"memory"); pin(r...); }
#define WAITV(n) waitv<n>()
#define WAITL() asm volatile("s_waitcnt lgkmcnt(0)":::"memory")
#define BARRIER() asm volatile("s_waitcnt lgkmcnt(0)\n\ts_barrier":::"memory")
DI void MFMA16  (f32x4& acc,const u32x4& a,const u32x4& b){ asm volatile("v_mfma_f32_16x16x32_bf16 %0, %1, %2, %0":"+v"(acc):"v"(a),"v"(b)); }
DI void MFMA16Z (f32x4& acc,const u32x4& a,const u32x4& b){ asm volatile("v_mfma_f32_16x16x32_bf16 %0, %1, %2, 0":"=&v"(acc):"v"(a),"v"(b)); }
DI void MFMA16A (f32x4& acc,const u32x4& a,const u32x4& b){ asm volatile("v_mfma_f32_16x16x32_bf16 %0, %1, %2, %0":"+v"(acc):"a"(a),"v"(b)); }
DI void MFMA16AZ(f32x4& acc,const u32x4& a,const u32x4& b){ asm volatile("v_mfma_f32_16x16x32_bf16 %0, %1, %2, 0":"=&v"(acc):"a"(a),"v"(b)); }
DI void NOP8(){ asm volatile("s_nop 7":::"memory"); __builtin_amdgcn_sched_barrier(0); }
DI float bflo(unsigned x){ return __uint_as_float(x<<16); }
DI float bfhi(unsigned x){ return __uint_as_float(x&0xffff0000u); }
DI unsigned cvtpk(float lo,float hi){ f32x2 v={lo,hi}; bf16x2 b=__builtin_convertvector(v,bf16x2); return __builtin_bit_cast(unsigned,b); }
DI float rbf(float x){ return bflo(cvtpk(x,x)); }
DI u32x4 cat(s16x4 a, s16x4 b){ s16x8 r=__builtin_shufflevector(a,b,0,1,2,3,4,5,6,7); return __builtin_bit_cast(u32x4,r); }
DI u32x4 cat2(u32x2 a,u32x2 b){ return __builtin_shufflevector(a,b,0,1,2,3); }
DI s16x4 trread(unsigned a){ return __builtin_amdgcn_ds_read_tr16_b64_v4i16((LAS s16x4*)(size_t)a); }
DI u32x4 lr128(unsigned a){ return *(const LAS u32x4*)(size_t)a; }
DI f32x4 lr128f(unsigned a){ return *(const LAS f32x4*)(size_t)a; }
DI u32x2 lr64v(unsigned a){ return *(const volatile LAS u32x2*)(size_t)a; }
DI void lw128(unsigned a,u32x4 v){ *(LAS u32x4*)(size_t)a=v; }
DI void lw128f(unsigned a,f32x4 v){ *(LAS f32x4*)(size_t)a=v; }
DI void lw64(unsigned a,u32x2 v){ *(LAS u32x2*)(size_t)a=v; }
DI unsigned opq(unsigned x){ asm volatile("":"+v"(x)); return x; }
template<int CTRL> DI float dpp_add(float v){ return v+__builtin_bit_cast(float,__builtin_amdgcn_update_dpp(0,__builtin_bit_cast(int,v),CTRL,0xf,0xf,true)); }
DI float sum32(float v){
  v=dpp_add<0x128>(v); v=dpp_add<0x124>(v); v=dpp_add<0x122>(v); v=dpp_add<0x121>(v);
  auto r=__builtin_amdgcn_permlane16_swap(__float_as_uint(v),__float_as_uint(v),false,false);
  return __uint_as_float(r[0])+__uint_as_float(r[1]); }

constexpr unsigned OOB=0xfff00000u;
template<int... I,class F> DI void sfor_impl(std::integer_sequence<int,I...>,F&& f){ (f(std::integral_constant<int,I>{}),...); }
template<int N,class F> DI void sfor(F&& f){ sfor_impl(std::make_integer_sequence<int,N>{},f); }
#define SFOR(N,v) sfor<N>([&](auto v##_) __attribute__((always_inline)) { constexpr int v=v##_;
#define SEND });
#define IC(v) std::integral_constant<int,(v)>{}
constexpr unsigned WS_E=0, WS_FP=256, WS_FQ=WS_FP+2048, WS_FH=WS_FQ+256, WS_FY=WS_FH+8192, WS_FLAGS_END=WS_FY+10240;
constexpr unsigned WS_ST=24576  , WS_CS=WS_ST+16384  , WS_UT=WS_CS+57344, WS_WDF=WS_UT+D_*S_*2, WS_HF=WS_WDF+F_*D_*2, WS_YT=WS_HF+S_*F_*2, WS_END=WS_YT+D_*S_*2;
constexpr unsigned WS_DBG=32u<<20;
static_assert(WS_FLAGS_END<=WS_ST,"");
constexpr int STG=49152;
constexpr int L_KV=2*STG, L_CSD=2*STG+16384;
constexpr int L_TAB=3*STG;
constexpr int CV_HC=4160, CV_H=4*CV_HC, CV_U=4608, CV_CH=CV_H+CV_U;
constexpr int L_CV=STG;
constexpr int L_RED=2*STG;
constexpr int L_Y=L_TAB;
constexpr int LDS_BYTES=163840;

#define STAMP(i)

#define STAMP2(i)
#define STAMPM(i) STAMP(i)
#define STAMP5(i)
#define STAMP4(i)
#define STAMP3(i)
#define STAMPL(i) STAMP(i)
DI void spin4(const u32x4& rws, unsigned off, int nlanes, u64 tag){
  const int lane=threadIdx.x&63; const unsigned vo=(off+lane*32)|(lane<nlanes?0:OOB); const unsigned tl=(unsigned)tag, th=(unsigned)(tag>>32);
  const u64 t0=__builtin_amdgcn_s_memrealtime();
  for(;;){
    u32x4 a,c; ld4s<0>(a,vo,rws,0); ld4s<16>(c,vo,rws,0); waitv<0>(a,c);
    const bool ok= a[0]==tl&&a[1]==th&&a[2]==tl&&a[3]==th&&c[0]==tl&&c[1]==th&&c[2]==tl&&c[3]==th;
    if(__all(ok || lane>=nlanes)) break;
    if(__builtin_amdgcn_s_memrealtime()-t0>2000000ull) break;
    __builtin_amdgcn_s_sleep(1);
  }
}
DI void put_flag(char* ws, unsigned off, u64 tag){ __hip_atomic_store((GAS u64*)(ws+off),tag,__ATOMIC_RELAXED,__HIP_MEMORY_SCOPE_AGENT); }
DI bool tag4(u32x4 v,u64 tag){ const unsigned tl=(unsigned)tag, th=(unsigned)(tag>>32); return v[0]==tl&&v[1]==th&&v[2]==tl&&v[3]==th; }

__global__ __launch_bounds__(256,1) void fused_kernel(const u16* __restrict__ x,const u16* __restrict__ kk,const u16* __restrict__ vv,const u16* __restrict__ sk,
    const u16* __restrict__ hk,const u16* __restrict__ Dv,const u16* __restrict__ wg,const u16* __restrict__ wu,const u16* __restrict__ wd,u16* __restrict__ out,char* ws){
  extern __shared__ __attribute__((aligned(16))) char smem[];
  STAMP(0);
  asm volatile("":::"a0","a255");
  asm volatile(""::"s"(x),"s"(kk),"s"(vv),"s"(sk),"s"(hk),"s"(Dv),"s"(wg),"s"(wu),"s"(wd),"s"(out),"s"(ws));
  const int tid=threadIdx.x, lane=tid&63; const unsigned w=rfl(tid>>6); const int b=blockIdx.x;
  const unsigned lds0=(unsigned)(size_t)(LAS char*)smem;
  const u32x4 rws=mk_rsrc(ws,WS_END), rx=mk_rsrc(x,S_*D_*2);
  u32x2 Eraw; { const unsigned z=0; LD2S(Eraw,z,rws,0,0); }
  const int lrow=8*b+2*w+(lane>>5), l32=lane&31;
  u32x4 xc3[3];
  { const unsigned vo=lrow*1280+l32*16; ld4p<NTX,0>(xc3[0],vo,rx,0); ld4p<NTX,512>(xc3[1],vo,rx,0); const unsigned v2=(vo+1024)|(l32<16?0:OOB); ld4p<NTX,0>(xc3[2],v2,rx,0); }
  STAMP3(6);
  constexpr int NPF1=0;
  constexpr int NPF=NPF1;
  auto pinpf=[&]() __attribute__((always_inline)) {
  };
  const int xcd=b&7, jj=b>>3; const bool isg=jj<28;
  const int g1id=xcd*28+jj, spid=xcd*4+jj-28;
  const int mi=(xcd>>1)*4+(jj&3), ni=(xcd&1)*7+(jj>>2);
  const int gj=lane&15,gq=lane>>4;
  const bool iskv=isg && g1id>=64; const int kvt=g1id-64;
  const int tb=kvt/5, cb=kvt-5*tb, t0=tb*64, pc_=tid&15, tg=tid>>4, d0=cb*128+pc_*8;
  STAMP3(7);
  const u32x4 rg=mk_rsrc(wg,D_*F_*2), ru=mk_rsrc(wu,D_*F_*2);
  unsigned vx[4], vw[4];
  #pragma unroll
  for(int i=0;i<4;i++){ const int row=32*w+8*i+(lane>>3); const int c=(lane&7)^((row>>1)&7); vx[i]=(128*mi+row)*1280+c*16-1024*i;
    const int k=16*w+4*i+(lane>>4), cp=lane&15, gs=(cp>>1)^((k&3)|(((k>>3)&1)<<2)); vw[i]=k*3456+ni*256+gs*32+(cp&1)*16-1024*i; }
  const unsigned m0x=rfl(lds0+w*4096), m0g=m0x+16384, m0u=m0x+32768;
  auto piece=[&](int P,int st,int bf) __attribute__((always_inline)) {
    if(P==0) SETM0(m0x+bf*STG); if(P==4) SETM0(m0g+bf*STG); if(P==8) SETM0(m0u+bf*STG);
    const unsigned sx=st*128, sw=st*221184;
    if(P==0) dmap<NTSX,0>(vx[0],rx,sx); if(P==1) dmap<NTSX,1024>(vx[1],rx,sx); if(P==2) dmap<NTSX,2048>(vx[2],rx,sx); if(P==3) dmap<NTSX,3072>(vx[3],rx,sx);
    if(P==4) dmap<NTSW,0>(vw[0],rg,sw); if(P==5) dmap<NTSW,1024>(vw[1],rg,sw); if(P==6) dmap<NTSW,2048>(vw[2],rg,sw); if(P==7) dmap<NTSW,3072>(vw[3],rg,sw);
    if(P==8) dmap<NTSW,0>(vw[0],ru,sw); if(P==9) dmap<NTSW,1024>(vw[1],ru,sw); if(P==10) dmap<NTSW,2048>(vw[2],ru,sw); if(P==11) dmap<NTSW,3072>(vw[3],ru,sw);
  };
  u64 tag;
  auto stats=[&]() __attribute__((always_inline)) {
  tag=((u64)rfl(Eraw[0])|((u64)rfl(Eraw[1])<<32))*6364136223846793005ull+1442695040888963407ull;
  {
    f32x2 f[12];
    #pragma unroll
    for(int i=0;i<3;i++) for(int e=0;e<4;e++) f[4*i+e]=f32x2{bflo(xc3[i][e]),bfhi(xc3[i][e])};
    f32x2 s0=(f[0]+f[1])+(f[2]+f[3]), s1=(f[4]+f[5])+(f[6]+f[7]), s2=(f[8]+f[9])+(f[10]+f[11]); s0=(s0+s1)+s2;
    const float mean=sum32(s0[0]+s0[1])*(1.f/640.f);
    f32x2 q0={0.f,0.f},q1={0.f,0.f},q2={0.f,0.f};
    #pragma unroll
    for(int i=0;i<4;i++){ f32x2 d=f[i]-mean, e=f[4+i]-mean, g=f[8+i]-mean; q0=d*d+q0; q1=e*e+q1; q2=g*g+q2; }
    q0+=q1;
    const float var=rbf(sum32(q0[0]+q0[1]+(l32<16? q2[0]+q2[1]:0.f))*(1.f/640.f)); const float m=rbf(mean);
    const float den=rbf(__builtin_amdgcn_sqrtf(rbf(var+1e-5f)));
    u32x2 sv={__float_as_uint(m),__float_as_uint(__builtin_amdgcn_rcpf(den))}; const unsigned vo=(WS_ST+lrow*8)|(l32==0?0:OOB);
    ST2S(sv,vo,rws,0,0);
  }
  };
  const int mt=(xcd>>1)*8+(jj&7), nt=(xcd&1)*4+(jj>>3);
  const u32x4 rhk=mk_rsrc(hk,D_*S_*2);
  const int cvp=w>>1, cvh=w&1;
  const unsigned ldsC=rfl(lds0+L_CV+cvp*CV_CH);
  const int chl=128+2*b+cvp;
  const unsigned vw0=lane*16, vw1=vw0+4096;
  const int kst=14*w-(w==3?1:0), ns= w<2? 14:13, rot=ROT*(((jj>>3)*ns)>>2);
  unsigned k1k[14];
  #pragma unroll
  for(int i=0;i<14;i++){ int t=i+rot; t= t>=ns? t-ns: t; k1k[i]=rfl((kst+t)*1024); }
  const unsigned wdbase=rfl(WS_WDF+nt*54*5*1024);
  auto ldwd=[&](auto NC) __attribute__((always_inline)) { constexpr int n=decltype(NC)::value, i=n/5, dt=n%5;
    const unsigned so=wdbase+5*k1k[i]; if constexpr(dt<4) ldwa<n,dt*1024>(vw0,rws,so); else ldwa<n,0>(vw1,rws,so); };
  auto conv_issue=[&](unsigned base,int ch,int part) __attribute__((always_inline)) {
    const unsigned so=rfl(ch*4096);
    if(part<2){ const int c=2*cvh+part; const unsigned vo=lane*16+2*c; SETM0(base+c*CV_HC+32); dman<0>(vo,rhk,so); dman<1024>(vo,rhk,so); dman<2048>(vo,rhk,so); dman<3072>(vo,rhk,so); }
    else { const unsigned vo=WS_UT+lane*16+cvh*2048; SETM0(base+CV_H+cvh*2048); DMAS(vo,rws,so,0); DMAS(vo,rws,so,1024); }
  };
  const unsigned ldsR0=rfl(lds0+cvp*CV_CH), ldsR1=rfl(lds0+2*STG+16384+cvp*CV_CH);
  constexpr int NWE=44;
  if(isg){
    auto middle=[&](auto NLC, unsigned& tk) __attribute__((always_inline)) { constexpr int NL=decltype(NLC)::value;
      { const unsigned vo=((128+2*b)*4096+lane*128)|((w==0&&TKM)?0:OOB); if constexpr(TKM==2) ld1n<0>(tk,vo,rhk,0); else LD1(tk,vo,rhk,0,0); }
      waitv<NL+1+NPF>(Eraw,xc3[0],xc3[1],xc3[2]);
      STAMP3(8);
      stats();
      STAMPM(1);
      #pragma unroll
      for(int P=0;P<12;P++) piece(P,0,0); };
    const unsigned P=lds0+L_KV;
    if(iskv){
      u32x4 pr[15];
      const u32x4 rk=mk_rsrc(kk,S_*D_*2), rv=mk_rsrc(vv,S_*D_*2), rsk=mk_rsrc(sk,D_*8);
      const unsigned vb=((t0+4*tg)*D_+d0)*2, va=(vb-3840)|((t0+tg==0)?OOB:0), vs=d0*8;
      ld4n<0>(pr[0],va,rk,0); ld4n<1280>(pr[1],va,rk,0); ld4n<2560>(pr[2],va,rk,0);
      ld4n<0>(pr[3],vb,rk,0); ld4n<1280>(pr[4],vb,rk,0); ld4n<2560>(pr[5],vb,rk,0); ld4n<3840>(pr[6],vb,rk,0);
      ld4n<0>(pr[7],vb,rv,0); ld4n<1280>(pr[8],vb,rv,0); ld4n<2560>(pr[9],vb,rv,0); ld4n<3840>(pr[10],vb,rv,0);
      ld4<0>(pr[11],vs,rsk,0); ld4<16>(pr[12],vs,rsk,0); ld4<32>(pr[13],vs,rsk,0); ld4<48>(pr[14],vs,rsk,0);
      unsigned vu=opq(WS_UT+(cb*128+(tid>>3))*4096+(S_-64-t0+(tid&7)*8)*2), lw_=opq(P+8*pc_*128+((15-tg)^pc_)*8);
      unsigned tk; middle(IC(15),tk);
      waitv<13>(pr[0],pr[1],pr[2],pr[3],pr[4],pr[5],pr[6],pr[7],pr[8],pr[9],pr[10],pr[11],pr[12],pr[13],pr[14],tk);
      pinpf();
      STAMP3(9);
      #pragma unroll
      for(int e2=0;e2<4;e2++){
        piece(3*e2,1,1); piece(3*e2+1,1,1); piece(3*e2+2,1,1);
        const f32x2 w0={bflo(pr[11+e2][0]),bflo(pr[11+e2][2])}, w1={bfhi(pr[11+e2][0]),bfhi(pr[11+e2][2])}, w2={bflo(pr[11+e2][1]),bflo(pr[11+e2][3])}, w3={bfhi(pr[11+e2][1]),bfhi(pr[11+e2][3])};
        f32x2 kf[7];
        #pragma unroll
        for(int i=0;i<7;i++) kf[i]=f32x2{bflo(pr[i][e2]),bfhi(pr[i][e2])};
        f32x2 o[4];
        #pragma unroll
        for(int i=0;i<4;i++){ f32x2 ks=kf[i]*w3; ks=kf[i+1]*w2+ks; ks=kf[i+2]*w1+ks; ks=kf[i+3]*w0+ks; o[i]=ks*f32x2{bflo(pr[7+i][e2]),bfhi(pr[7+i][e2])}; }
        #pragma unroll
        for(int z=0;z<2;z++){ u32x2 pk={cvtpk(o[3][z],o[2][z]),cvtpk(o[1][z],o[0][z])}; lw64(lw_+(2*e2+z)*128,pk); }
      }
      STAMP3(10);
      BARRIER();
      STAMP3(11);
      u32x4 v[4];
      #pragma unroll
      for(int i=0;i<4;i++) v[i]=lr128(P+((tid>>3)+32*i)*128+(((tid&7)^((w>>1)+2*i))*16));
      #pragma unroll
      for(int i=0;i<4;i++){ const u32x4 o= (w&1)? u32x4{v[i][2],v[i][3],v[i][0],v[i][1]} : v[i]; st4s<0>(o,vu,rws,i*131072); }
    } else {
      u32x4 pr[12];
      const u32x4 rwd=mk_rsrc(wd,F_*D_*2);
      const unsigned vo=(lane>>3)*1280+(lane&7)*16; unsigned so[3], sd[3]; int c4[3];
      #pragma unroll
      for(int z=0;z<3;z++){ const int un=4*g1id+w+256*z, s=un/10, c64=un-10*s; so[z]=rfl(un<540? (32*s*D_+c64*64)*2 : 0x10000000); sd[z]=rfl(un<540? WS_WDF+s*5120 : 0x10000000); c4[z]=4*c64; }
      SFOR(12,i) ld4n<0>(pr[i],vo,rwd,so[i>>2]+(i&3)*8*1280); SEND
      unsigned tk; middle(IC(12),tk);
      waitv<13>(pr[0],pr[1],pr[2],pr[3],pr[4],pr[5],pr[6],pr[7],pr[8],pr[9],pr[10],pr[11],tk);
      pinpf();
      const unsigned L=P+w*12288;
      #pragma unroll
      for(int z=0;z<3;z++){
        #pragma unroll
        for(int i=0;i<4;i++){ piece(4*z+i,1,1); const int r=8*i+(lane>>3); lw128(L+z*4096+r*128+(((lane&7)^((r>>2)&7))*16),pr[4*z+i]); } }
      BARRIER();
      const int qq=(lane&15)>>2, p=lane&3, r=4*gq+qq;
      SFOR(12,i) constexpr int z=i>>2, f4=i&3; const int ch=2*f4+(p>>1);
        const u32x4 v=cat(trread(L+z*4096+r*128+((ch^((r>>2)&7))*16)+(p&1)*8),trread(L+z*4096+(r+16)*128+((ch^(((r+16)>>2)&7))*16)+(p&1)*8));
        const int D16=c4[z]+f4, n5=D16/5, dt=D16-5*n5; st4s<0>(v,vw0,rws,rfl(sd[z]+n5*276480+dt*1024)); SEND
    }
    STAMPM(2);
    const int wm=w>>1, wn=w&1;
    unsigned xa[2], wa[4];
    { const int qq=(lane&15)>>2, p=lane&3;
      #pragma unroll
      for(int j=0;j<2;j++) xa[j]=opq(lds0+(64*wm+gj)*128+(((4*j+gq)^((lane>>1)&7))*16));
      #pragma unroll
      for(int z=0;z<4;z++) wa[z]=opq(lds0+16384+(8*gq+qq)*256+(((4*wn+z)^(qq|((gq&1)<<2)))*32)+p*8); }
    f32x4 ag[4][4], au[4][4];
    u32x4 fw[4], fx[2][4];
    u32x4 fp0,fp1; u32x2 fq, st[4]; unsigned csp[2];
    auto rdw=[&](auto GC,auto JC,auto ZC,auto HC) __attribute__((always_inline)) {
      constexpr int G=decltype(GC)::value, J=decltype(JC)::value, Z=decltype(ZC)::value, Hh=decltype(HC)::value;
      const u32x2 t=__builtin_bit_cast(u32x2,trread(wa[Z&3]+J*8192+(Z>>2)*16384+Hh*1024));
      if constexpr(Hh==0){ u32x4 v={t[0],t[1],t[0],t[1]}; fw[G&3]=v; } else { fw[G&3][2]=t[0]; fw[G&3][3]=t[1]; } };
        WAITV(16); BARRIER();
    #pragma unroll
    for(int m=0;m<4;m++) fx[0][m]=lr128(xa[0]+m*2048);
    SFOR(3,z) rdw(IC(z),IC(0),IC(z),IC(0)); rdw(IC(z),IC(0),IC(z),IC(1)); SEND
    STAMPM(3); STAMP5(1);
    SFOR(10,s)
      SFOR(2,j)
        constexpr int c=j&1,n=c^1;
        SFOR(8,z)
          constexpr int G=(2*s+j)*8+z;
          SFOR(4,m)
            f32x4& A= z<4? ag[z&3][m] : au[z&3][m];
            if constexpr(s==0&&j==0) MFMA16Z(A,fw[G&3],fx[c][m]); else MFMA16(A,fw[G&3],fx[c][m]);
            if constexpr(j==0){
              if constexpr(m<2){ if constexpr(z<5) rdw(IC(G+3),IC(0),IC(z+3),IC(m)); else rdw(IC(G+3),IC(1),IC(z-5),IC(m)); }
              if constexpr(m==2 && z>=4) fx[n][z-4]=lr128(xa[1]+(z-4)*2048);
            } else {
              if constexpr(z<5 && m<2) rdw(IC(G+3),IC(1),IC(z+3),IC(m));
              if constexpr(s<9 && z==6){ if constexpr(m<2){ rdw(IC(G+2),IC(0),IC(0),IC(m)); fx[n][m]=lr128(xa[0]+m*2048); } else rdw(IC(G+3),IC(0),IC(1),IC(m-2)); }
              if constexpr(s<9 && z==7 && m<2){ rdw(IC(G+3),IC(0),IC(2),IC(m)); fx[n][2+m]=lr128(xa[0]+(2+m)*2048); }
            }
            if constexpr(s==0&&j==0){ if constexpr(z<6 && (m&1)) piece(2*z+(m>>1),2,2); }
            else if constexpr(m==3){
              if constexpr(j==1 && z>=6 && s+3<10) piece(z-6,s+3,(s+3)%3);
              if constexpr(j==0 && s+2<10) piece(2+z,s+2,(s+2)%3);
              if constexpr(j==1 && z<2 && s>=1 && s+2<10) piece(10+z,s+2,(s+2)%3);
              if constexpr(s==7 && j==1 && z==2){ const unsigned vo=WS_FP+lane*32; LD4S(fp0,vo,rws,0,0); LD4S(fp1,vo,rws,0,16);
                const unsigned vq=(WS_FQ+lane*8)|(lane<32?0:OOB); LD2S(fq,vq,rws,0,0); }
              if constexpr(s==8 && j==0 && (z==1||z==5)) conv_issue(ldsC,chl,z>>2);
            }
            GAP();
          SEND
          if constexpr(j==1 && z==5 && s<9){
            if constexpr(s==0) WAITV(16); else if constexpr(s<=6) WAITV(12); else if constexpr(s==7) WAITV(15); else WAITV(8);
            if constexpr(s==8){
              pin(fp0,fp1,fq);
              if(!__all(tag4(fp0,tag)&&tag4(fp1,tag)&&((fq[0]==(unsigned)tag&&fq[1]==(unsigned)(tag>>32))||lane>=32))){
                spin4(rws,WS_FP,64,tag); spin4(rws,WS_FQ,8,tag); }
              if(b==0 && tid==0) put_flag(ws,WS_E,tag);
              #pragma unroll
              for(int m=0;m<4;m++){ const unsigned vo=WS_ST+(128*mi+64*wm+16*m+gj)*8; LD2S(st[m],vo,rws,0,0); }
              const int col= tid<128? ni*128+tid : 1728+ni*128+tid-128; const unsigned vo=(WS_CS+col*4)|(((tid&127)+ni*128<1728)?0:OOB);
              LD1S(csp[0],vo,rws,0,0); { const unsigned so=13824; LD1S(csp[1],vo,rws,so,0); }
              conv_issue(ldsC,chl,2);
            }
            BARRIER();
            STAMP5(s+2);
            if constexpr(s==1) if(tid==0) put_flag(ws,WS_FP+b*8,tag);
            constexpr int dl= (s+1)%3==0? -2*STG : STG;
            xa[0]=opq(xa[0]+dl); xa[1]=opq(xa[1]+dl); wa[0]=opq(wa[0]+dl); wa[1]=opq(wa[1]+dl); wa[2]=opq(wa[2]+dl); wa[3]=opq(wa[3]+dl);
            GAP();
          }
        SEND
      SEND
    SEND
    STAMPM(4); STAMP5(11);
    waitv<2>(csp[0],csp[1],st[0],st[1],st[2],st[3]);
    *(LAS float*)(size_t)(lds0+L_TAB+tid*4)=__uint_as_float(csp[0])+__uint_as_float(csp[1]);
    BARRIER();
    NOP8();
    { const float L2E=1.4426950408889634f;
      f32x4 cg[4], cu[4];
      #pragma unroll
      for(int z=0;z<4;z++){ cg[z]=lr128f(lds0+L_TAB+(64*wn+16*z+4*gq)*4); cu[z]=lr128f(lds0+L_TAB+512+(64*wn+16*z+4*gq)*4); }
      SFOR(4,i) ldwd(IC(i)); SEND
      SFOR(4,m)
        const float mean=__uint_as_float(st[m][0]), r=__uint_as_float(st[m][1]);
        const float R=-L2E*r, C=-r*(1.f/L2E), nm=-mean;
        SFOR(2,G)
          u32x4 o;
          SFOR(4,te)
            constexpr int T=te>>1, e=te&1, z=2*G+T;
            f32x2 g={ag[z][m][2*e],ag[z][m][2*e+1]}, u={au[z][m][2*e],au[z][m][2*e+1]};
            g=f32x2{cg[z][2*e],cg[z][2*e+1]}*nm+g; u=f32x2{cu[z][2*e],cu[z][2*e+1]}*nm+u;
            f32x2 s=g*R; f32x2 ex={__builtin_amdgcn_exp2f(s[0]),__builtin_amdgcn_exp2f(s[1])};
            f32x2 d=ex+1.f; f32x2 rc={__builtin_amdgcn_rcpf(d[0]),__builtin_amdgcn_rcpf(d[1])};
            f32x2 h=((u*C)*s)*rc; o[2*T+e]=cvtpk(h[0],h[1]);
            ldwd(IC(4+(m*2+G)*5+te));
            GAP();
          SEND
          const int s32=ni*4+2*wn+G, m16=mi*8+wm*4+m;
          const unsigned vo=(WS_HF+((m16*54+s32)*64+lane)*16)|(s32<54?0:OOB); ST4S(o,vo,rws,0,0);
          ldwd(IC(4+(m*2+G)*5+4));
        SEND
      SEND }
    STAMPM(5);
    WAITV(52);
  } else {
    unsigned tk0;
    {
      const int gw=4*spid+w, cbk=gw>>1, kh=gw&1; const bool act=gw<108;
      const u32x4 rcs=mk_rsrc(cbk<27? wg:wu, D_*F_*2); const int cb64=cbk<27? cbk: cbk-27;
      unsigned vc[2];
      #pragma unroll
      for(int i=0;i<2;i++){ const int r=8*i+(lane>>3), cp=lane&7; const int gs=(cp>>1)^(((r>>1)&1)|((i&1)<<1)); vc[i]=((320*kh+r)*3456+cb64*128+gs*32+(cp&1)*16)|(act?0:OOB); }
      const unsigned LB=rfl(lds0+w*16384);
      auto csissue=[&](int c) __attribute__((always_inline)) {
        #pragma unroll
        for(int i=0;i<8;i++){ SETM0(LB+(c&1)*8192+i*1024); const unsigned so=(64*c+16*(i>>1))*3456; dmap<NTCS,0>(vc[i&1],rcs,so); } };
      { const unsigned vo=((w<2? 4*spid+2*w : 128+2*b)*4096+lane*128)|((w<3&&TKM)?0:OOB); if constexpr(TKM==2) ld1n<0>(tk0,vo,rhk,0); else LD1(tk0,vo,rhk,0,0); }
      csissue(0); csissue(1);
      waitv<17+NPF>(Eraw,xc3[0],xc3[1],xc3[2]);
      stats();
      STAMPM(1);
      WAITV(0); pinpf();
      BARRIER();
      if(tid==0) put_flag(ws,WS_FP+b*8,tag);
      const int qq=(lane&15)>>2, p=lane&3; f32x4 cs[4]; u32x4 on={0x3f803f80u,0x3f803f80u,0x3f803f80u,0x3f803f80u}; asm volatile("s_nop 1":"+v"(on));
      SFOR(5,c)
        if constexpr(c>=1 && c<4) WAITV(8); else if constexpr(c==4) WAITV(0);
        u32x4 fa[2][4];
        SFOR(2,k) SFOR(4,z) const int r0=32*k+8*gq+qq; const int g0=((r0>>1)&1)|(((r0>>3)&1)<<1);
          fa[k][z]=cat(trread(LB+(c&1)*8192+r0*128+((z^g0)*32)+p*8),trread(LB+(c&1)*8192+(r0+4)*128+((z^g0)*32)+p*8)); SEND SEND
        WAITL();
        if constexpr(c+2<5) csissue(c+2);
        GAP();
        SFOR(2,k) SFOR(4,z) if constexpr(c==0&&k==0) MFMA16Z(cs[z],fa[k][z],on); else MFMA16(cs[z],fa[k][z],on); SEND SEND
        GAP();
      SEND
      NOP8(); pin(cs[0],cs[1],cs[2],cs[3]);
      f32x4 v;
      #pragma unroll
      for(int e=0;e<4;e++) v[e]= gj==0? cs[0][e] : gj==1? cs[1][e] : gj==2? cs[2][e] : cs[3][e];
      const unsigned vo=(WS_CS+(kh*3456+cbk*64+16*gj+4*gq)*4)|((act&&gj<4)?0:OOB); ST4S(__builtin_bit_cast(u32x4,v),vo,rws,0,0);
    }
    STAMPM(3);
    BARRIER();
    conv_issue(ldsR0,4*spid+cvp,0); conv_issue(ldsR0,4*spid+cvp,1);
    conv_issue(ldsR1,4*spid+2+cvp,0); conv_issue(ldsR1,4*spid+2+cvp,1);
    conv_issue(ldsC,chl,0); conv_issue(ldsC,chl,1);
    waitv<24>(tk0); BARRIER();
    if(tid==0) put_flag(ws,WS_FQ+spid*8,tag);
    STAMPM(4);
    STAMPM(5);
  }
  u32x2 ph, xv[5], stv;
  const int trow=64*mt+16*w+gj;
  u32x4 H[14][4];
  unsigned vh[4];
  #pragma unroll
  for(int r=0;r<4;r++) vh[r]=WS_HF+((w+r)&3)*54*1024+lane*16;
  auto hoff=[&](int i) __attribute__((always_inline)) ->unsigned { return (i<13||w<2)? (unsigned)(4*mt*54*1024)+k1k[i] : 0x10000000u; };
  auto ldH=[&](int i) __attribute__((always_inline)) {
    const unsigned sh= (i<13||w<2)? (unsigned)(4*mt*54*1024)+k1k[i] : 0x10000000u;

    LD4S(H[i][0],vh[0],rws,sh,0); LD4S(H[i][1],vh[1],rws,sh,0); LD4S(H[i][2],vh[2],rws,sh,0); LD4S(H[i][3],vh[3],rws,sh,0); };
  auto conv_round=[&](auto LASTC, const unsigned base, const int ch) __attribute__((always_inline)) {
    constexpr bool LAST=decltype(LASTC)::value;
    BARRIER();
    if(cvh==0){
      const int c=lane>>4,p=lane&15; const LAS u16* src=(const LAS u16*)(size_t)(base+32+ (p>=16-c? (p-16+c)*2:0));
      u16 v=*src; *(LAS u16*)(size_t)(base+c*CV_HC+p*2)= p>=16-c? v:(u16)0;
    } else if(lane<32) lw128(base+CV_H+4096+lane*16,u32x4{0,0,0,0});
    const float Dd=bflo(((const unsigned*)Dv)[ch>>1]>>((ch&1)*16));
    BARRIER();
    if constexpr(LAST){ STAMPL(6); }
    const int ci=gj,cq=gq,cc=(ci+1)&3;
    const unsigned pa=opq(base+cc*CV_HC+2*(1+ci-cc+8*cq)+256*cvh), pb=opq(base+CV_H+2*(2032-16*ci+8*cq)-3584+256*cvh);
    f32x4 ya[8];
    { u32x4 B[2][8], A[2][8];
      auto ldA=[&](int rho,int a)->u32x4{ const unsigned p=pa+64*rho+512*a; return cat2(lr64v(p),lr64v(p+8)); };
      auto ldB=[&](int rho,int bb)->u32x4{ return lr128(pb+64*rho+512*(7-bb)); };
      #pragma unroll
      for(int z=0;z<8;z++){ B[0][z]=ldB(0,z); A[0][z]=ldA(0,z); }
      GAP();
      SFOR(4,rho)
        constexpr int c=rho&1,n=c^1;
        if constexpr(LAST && rho==2){
          WAITV(15); if(isg && lane==0) put_flag(ws,WS_FH+(mi*64+ni*4+w)*8,tag); }
        if constexpr(LAST && rho==3){ const unsigned vo=(WS_FH+((mt>>1)*64+lane)*8)|(lane<56?0:OOB); LD2S(ph,vo,rws,0,0); }
        SFOR(8,a)
          SFOR(8-a,nn_)
            constexpr int nu=a+nn_; constexpr int cnt=a*8-(a*(a-1))/2+(nu-a);
            if constexpr(rho==0 && a==0) MFMA16Z(ya[nu],A[c][a],B[c][nu-a]); else MFMA16(ya[nu],A[c][a],B[c][nu-a]);
            if constexpr(rho<3){
              if constexpr(cnt<8) B[n][cnt]=ldB(rho+1,cnt);
              else if constexpr(cnt<24){ constexpr int az=(cnt-8)>>1; const u32x2 t=lr64v(pa+64*(rho+1)+512*az+8*(cnt&1));
                if constexpr((cnt&1)==0){ u32x4 v={t[0],t[1],t[0],t[1]}; A[n][az]=v; } else { A[n][az][2]=t[0]; A[n][az][3]=t[1]; } } }
            if constexpr(LAST && rho<3 && cnt>=24 && cnt<31){ constexpr int nn=NWE+rho*7+(cnt-24); if constexpr(nn<64) ldwd(IC(nn)); }
            if constexpr(LAST && rho==3 && cnt>=4 && cnt<28 && cnt%4==0){
              constexpr int q=cnt/4-1; const unsigned vo=(trow*D_+80*nt+4*gq)*2;
              if constexpr(q<5) ld2<32*q>(xv[q],vo,rx,0); else { const unsigned vs=WS_ST+trow*8; LD2S(stv,vs,rws,0,0); } }
            GAP();
          SEND
        SEND
      SEND
    }
    if constexpr(LAST){ STAMPL(7);
      waitv<6>(ph);
      if(!__all((ph[0]==(unsigned)tag && ph[1]==(unsigned)(tag>>32)) || lane>=56)) spin4(rws,WS_FH+(mt>>1)*512,14,tag);
      ldH(0); ldH(1); ldH(2); ldH(3); }
    NOP8();
    f32x4 mine[4];
    { const unsigned R=lds0+L_RED+w*4096+lane*16;
      #pragma unroll
      for(int z=0;z<4;z++){ const f32x4 lo=ya[z], hi=ya[4+z]; f32x4 snd, kp;
        #pragma unroll
        for(int e=0;e<4;e++){ snd[e]= cvh? lo[e]:hi[e]; kp[e]= cvh? hi[e]:lo[e]; }
        lw128f(R+z*1024,snd); mine[z]=kp; } }
    BARRIER();
    { const unsigned R=lds0+L_RED+(w^1)*4096+lane*16; unsigned pk[4][2];
      #pragma unroll
      for(int z=0;z<4;z++){ const f32x4 y=mine[z]+lr128f(R+z*1024);
        const int t=256*(4*cvh+z)+16*ci+4*cq;
        const u32x2 uu=*(const LAS u32x2*)(size_t)(base+CV_H+2*(2044-t));
        pk[z][0]=cvtpk(fmaf(bfhi(uu[1]),Dd,y[0]),fmaf(bflo(uu[1]),Dd,y[1])); pk[z][1]=cvtpk(fmaf(bfhi(uu[0]),Dd,y[2]),fmaf(bflo(uu[0]),Dd,y[3])); }
      #pragma unroll
      for(int z=0;z<4;z+=2){
        auto r0=__builtin_amdgcn_permlane16_swap(pk[z][0],pk[z+1][0],false,false);
        auto r1=__builtin_amdgcn_permlane16_swap(pk[z][1],pk[z+1][1],false,false);
        u32x4 v={r0[0],r1[0],r0[1],r1[1]};
        const int t=256*(4*cvh+z+(cq&1))+16*ci+8*(cq>>1);
        const unsigned vo=WS_YT+ch*4096+t*2; ST4S(v,vo,rws,0,0); }
    }
  };
  if(!isg){
    spin4(rws,WS_FP,64,tag);
    conv_issue(ldsR0,4*spid+cvp,2); conv_issue(ldsR1,4*spid+2+cvp,2); conv_issue(ldsC,chl,2);
    unsigned tw[9];
    SFOR(9,i) const unsigned li=((jj-28)*4+w)*540+i*64+lane; const unsigned vt=(WS_WDF+(xcd&1)*4*276480+li*128)|((i*64+lane<540)?0:OOB); ld1s<0>(tw[i],vt,rws,0); SEND
    WAITV(13);
    STAMP4(6);
    conv_round(std::integral_constant<bool,false>{},ldsR0,4*spid+cvp);
    STAMP4(7);
    WAITV(13);
    conv_round(std::integral_constant<bool,false>{},ldsR1,4*spid+2+cvp);
    STAMP4(8);
    waitv<2>(tw[0],tw[1],tw[2],tw[3],tw[4],tw[5],tw[6],tw[7],tw[8]);
    if(lane==0) put_flag(ws,WS_FY+((4*spid+cvp)*2+cvh)*8,tag);
    spin4(rws,WS_FQ,8,tag);
    if(lane==0) put_flag(ws,WS_FY+((4*spid+2+cvp)*2+cvh)*8,tag);
    STAMP4(9);
    SFOR(NWE,n) ldwd(IC(n)); SEND
    STAMP4(10);
  }
  conv_round(std::integral_constant<bool,true>{},ldsC,chl);
  STAMPL(8);
  u32x4 WV[6]; u32x4 py0,py1; u32x4 yv[3];
  f32x4 oa[4][5];
  #define HREGS(i) H[i][0],H[i][1],H[i][2],H[i][3]
  SFOR(14,i)
    if constexpr(i==0){ waitv<14>(HREGS(0),xv[0],xv[1],xv[2],xv[3],xv[4],stv); }
    if constexpr(i==1){ waitv<14>(HREGS(1)); STAMP2(1);
                        }
    if constexpr(i==2) waitv<14>(HREGS(2));
    if constexpr(i==3){ waitv<12>(HREGS(3)); STAMP2(2);
                        if(lane==0) put_flag(ws,WS_FY+(chl*2+cvh)*8,tag); }
    if constexpr(i==4) waitv<12>(HREGS(4));
    if constexpr(i==5) waitv<18>(HREGS(5));
    if constexpr(i==6) waitv<20>(HREGS(6));
    if constexpr(i==7){ waitv<20>(HREGS(7)); STAMP2(3); }
    if constexpr(i==8) waitv<14>(HREGS(8),WV[0],WV[1],WV[2],WV[3],WV[4],WV[5]);
    if constexpr(i==9){ waitv<12>(HREGS(9),py0,py1); STAMP2(4);
              if(!__all((tag4(py0,tag)&&tag4(py1,tag))||lane>=40)){
                spin4(rws,WS_FY+160*nt*8,40,tag); } }
    if constexpr(i==10){ waitv<12>(HREGS(10)); STAMP2(5); }
    if constexpr(i==11) waitv<11>(HREGS(11));
    if constexpr(i==12) waitv<7>(HREGS(12));
    if constexpr(i==13) waitv<3>(HREGS(13));
    GAP();
    SFOR(4,tt)
      SFOR(5,dt)
        constexpr int nn=5*i+dt, sl=tt*5+dt;
        if constexpr(i==0) mfmawz<nn>(oa[tt][dt],H[0][tt]);
        else if constexpr(nn<64) mfmaw<nn>(oa[tt][dt],H[i][tt]);
        else MFMA16(oa[tt][dt],WV[nn-64],H[i][tt]);
        if constexpr(i+4<14 && sl%4==1 && sl<16){ const unsigned sh=hoff(i+4); ld4s<0>(H[i+4][sl>>2],vh[sl>>2],rws,sh); }
        if constexpr(i==4 && sl%3==0 && sl>=2 && sl<20){ constexpr int z=sl/3-1; if constexpr(z<6){ constexpr int n2=64+z, i2=n2/5, d2=n2%5;
            const unsigned so= (i2<13||w<2)? wdbase+5*k1k[i2]+d2*1024 : 0x10000000u; ld4s<0>(WV[z],vw0,rws,so); } }
        if constexpr(i==5 && sl==17){ const unsigned vo=(WS_FY+(160*nt+4*lane)*8)|(lane<40?0:OOB); LD4S(py0,vo,rws,0,0); LD4S(py1,vo,rws,0,16); }
        if constexpr(i==10 && sl%6==2){ constexpr int z=sl/6; const int id=tid+256*z; const int r=id>>3,c8=id&7;
            const unsigned vo=(WS_YT+(80*nt+r)*4096+(64*mt+c8*8)*2)|(id<640?0:OOB); LD4S(yv[z],vo,rws,0,0); }
        GAP();
      SEND
    SEND
  SEND
  STAMPL(9);
  waitv<0>(yv[0],yv[1],yv[2]);
  #pragma unroll
  for(int z=0;z<3;z++){ const int id=tid+256*z; if(id<640) lw128(lds0+L_Y+(id>>3)*160+(id&7)*16,yv[z]); }
  BARRIER();
  NOP8();
  #pragma unroll
  for(int r=1;r<4;r++){ const unsigned a=lds0+((((w+r)&3)*3+(r-1))*5)*1024+lane*16;
    #pragma unroll
    for(int dt=0;dt<5;dt++) lw128f(a+dt*1024,oa[r][dt]); }
  BARRIER();
  STAMPL(10);
  { const float m=__uint_as_float(stv[0]), rd=__uint_as_float(stv[1]); const unsigned a=lds0+(w*15)*1024+lane*16;
    #pragma unroll
    for(int dt=0;dt<5;dt++){
      f32x4 f=(oa[0][dt]+lr128f(a+dt*1024))+(lr128f(a+(5+dt)*1024)+lr128f(a+(10+dt)*1024));
      const u32x2 y2=__builtin_bit_cast(u32x2,trread(lds0+L_Y+(16*dt+4*gq+((lane&15)>>2))*160+(16*w+4*(lane&3))*2));
      const float xf[4]={bflo(xv[dt][0]),bfhi(xv[dt][0]),bflo(xv[dt][1]),bfhi(xv[dt][1])};
      const float yf[4]={bflo(y2[0]),bfhi(y2[0]),bflo(y2[1]),bfhi(y2[1])};
      float o[4];
      #pragma unroll
      for(int e=0;e<4;e++) o[e]=fmaf((xf[e]-m)*rd,yf[e],xf[e])+f[e];
      u32x2 ov={cvtpk(o[0],o[1]),cvtpk(o[2],o[3])};
      *(u32x2*)(out+(size_t)trow*D_+80*nt+16*dt+4*gq)=ov;
    } }
  STAMPL(11);
}

extern "C" void kernel_launch(void* const* d_in, const int* in_sizes, int n_in, void* d_out, int out_size, void* d_ws, size_t ws_size, hipStream_t stream){
  (void)hipFuncSetAttribute((const void*)fused_kernel,hipFuncAttributeMaxDynamicSharedMemorySize,LDS_BYTES);
  fused_kernel<<<256,256,LDS_BYTES,stream>>>((const u16*)d_in[0],(const u16*)d_in[1],(const u16*)d_in[2],(const u16*)d_in[3],(const u16*)d_in[4],(const u16*)d_in[5],
      (const u16*)d_in[7],(const u16*)d_in[8],(const u16*)d_in[9],(u16*)d_out,(char*)d_ws);
}
```

```cpp
#include <hip/hip_runtime.h>
#include <hip/hip_bf16.h>
#include <stdint.h>
#include <type_traits>
#include <utility>
#define LAS __attribute__((address_space(3)))
#define GAS __attribute__((address_space(1)))
#define DI __device__ __forceinline__
typedef unsigned short u16;
typedef unsigned long long u64;
typedef short s16x4 __attribute__((ext_vector_type(4)));
typedef short s16x8 __attribute__((ext_vector_type(8)));
typedef __bf16 bf16x2 __attribute__((ext_vector_type(2)));
typedef float f32x2 __attribute__((ext_vector_type(2)));
typedef float f32x4 __attribute__((ext_vector_type(4)));
typedef unsigned u32x4 __attribute__((ext_vector_type(4)));
typedef unsigned u32x2 __attribute__((ext_vector_type(2)));
constexpr int S_=2048, D_=640, F_=1728;
#define ROT 0
#define NTX 0
#define NTSX 0
#define NTSW 0
#define NTCS 0
#define TKM 0
#define PORD 0
#define GAP() __builtin_amdgcn_sched_barrier(0)
DI unsigned rfl(unsigned x){ return __builtin_amdgcn_readfirstlane(x); }
DI u32x4 mk_rsrc(const void* p, unsigned bytes){
  unsigned long long a=(unsigned long long)p;
  u32x4 r; r[0]=rfl((unsigned)a); r[1]=rfl((unsigned)(a>>32))&0xffffu; r[2]=bytes; r[3]=0x00020000u; return r;
}
DI void setm0(unsigned x){ asm volatile("s_mov_b32 m0, %0\n\ts_nop 0"::"s"(x)); }
template<int I> DI void dma (unsigned vo,const u32x4& r,unsigned so){ asm volatile("buffer_load_dwordx4 %0, %1, %2 offen offset:%3 lds"::"v"(vo),"s"(r),"s"(so),"n"(I)); }
template<int I> DI void dmas(unsigned vo,const u32x4& r,unsigned so){ asm volatile("buffer_load_dwordx4 %0, %1, %2 offen offset:%3 sc1 lds"::"v"(vo),"s"(r),"s"(so),"n"(I)); }
template<int I> DI void ld4  (u32x4& d,unsigned vo,const u32x4& r,unsigned so){ asm volatile("buffer_load_dwordx4 %0, %1, %2, %3 offen offset:%4":"=v"(d):"v"(vo),"s"(r),"s"(so),"n"(I)); }
template<int I> DI void dman(unsigned vo,const u32x4& r,unsigned so){ asm volatile("buffer_load_dwordx4 %0, %1, %2 offen offset:%3 nt lds"::"v"(vo),"s"(r),"s"(so),"n"(I)); }
template<int I> DI void ld4n (u32x4& d,unsigned vo,const u32x4& r,unsigned so){ asm volatile("buffer_load_dwordx4 %0, %1, %2, %3 offen offset:%4 nt":"=v"(d):"v"(vo),"s"(r),"s"(so),"n"(I)); }
template<int N,int I> DI void ldwa(unsigned vo,const u32x4& r,unsigned so){ asm volatile("buffer_load_dwordx4 a[%c3:%c4], %0, %1, %2 offen offset:%5 sc1"::"v"(vo),"s"(r),"s"(so),"n"(4*N),"n"(4*N+3),"n"(I)); }
template<int N> DI void mfmaw (f32x4& acc,const u32x4& b){ asm volatile("v_mfma_f32_16x16x32_bf16 %0, a[%c2:%c3], %1, %0":"+v"(acc):"v"(b),"n"(4*N),"n"(4*N+3)); }
template<int N> DI void mfmawz(f32x4& acc,const u32x4& b){ asm volatile("v_mfma_f32_16x16x32_bf16 %0, a[%c2:%c3], %1, 0":"=&v"(acc):"v"(b),"n"(4*N),"n"(4*N+3)); }
template<int NT,int I> DI void dmap(unsigned vo,const u32x4& r,unsigned so){ if constexpr(NT) dman<I>(vo,r,so); else dma<I>(vo,r,so); }
template<int NT,int I> DI void ld4p(u32x4& d,unsigned vo,const u32x4& r,unsigned so){ if constexpr(NT) ld4n<I>(d,vo,r,so); else ld4<I>(d,vo,r,so); }
template<int I> DI void ld1n (unsigned& d,unsigned vo,const u32x4& r,unsigned so){ asm volatile("buffer_load_dword %0, %1, %2, %3 offen offset:%4 nt":"=v"(d):"v"(vo),"s"(r),"s"(so),"n"(I)); }
template<int I> DI void ld4s (u32x4& d,unsigned vo,const u32x4& r,unsigned so){ asm volatile("buffer_load_dwordx4 %0, %1, %2, %3 offen offset:%4 sc1":"=v"(d):"v"(vo),"s"(r),"s"(so),"n"(I)); }
template<int I> DI void ld4sa(u32x4& d,unsigned vo,const u32x4& r,unsigned so){ asm volatile("buffer_load_dwordx4 %0, %1, %2, %3 offen offset:%4 sc1":"=a"(d):"v"(vo),"s"(r),"s"(so),"n"(I)); }
template<int I> DI void ld2s (u32x2& d,unsigned vo,const u32x4& r,unsigned so){ asm volatile("buffer_load_dwordx2 %0, %1, %2, %3 offen offset:%4 sc1":"=v"(d):"v"(vo),"s"(r),"s"(so),"n"(I)); }
template<int I> DI void ld2  (u32x2& d,unsigned vo,const u32x4& r,unsigned so){ asm volatile("buffer_load_dwordx2 %0, %1, %2, %3 offen offset:%4":"=v"(d):"v"(vo),"s"(r),"s"(so),"n"(I)); }
template<int I> DI void ld1s (unsigned& d,unsigned vo,const u32x4& r,unsigned so){ asm volatile("buffer_load_dword %0, %1, %2, %3 offen offset:%4 sc1":"=v"(d):"v"(vo),"s"(r),"s"(so),"n"(I)); }
template<int I> DI void ld1  (unsigned& d,unsigned vo,const u32x4& r,unsigned so){ asm volatile("buffer_load_dword %0, %1, %2, %3 offen offset:%4":"=v"(d):"v"(vo),"s"(r),"s"(so),"n"(I)); }
template<int I> DI void st4s (const u32x4& d,unsigned vo,const u32x4& r,unsigned so){ asm volatile("buffer_store_dwordx4 %0, %1, %2, %3 offen offset:%4 sc1\n\ts_nop 1"::"v"(d),"v"(vo),"s"(r),"s"(so),"n"(I)); }
template<int I> DI void st2s (const u32x2& d,unsigned vo,const u32x4& r,unsigned so){ asm volatile("buffer_store_dwordx2 %0, %1, %2, %3 offen offset:%4 sc1\n\ts_nop 1"::"v"(d),"v"(vo),"s"(r),"s"(so),"n"(I)); }
#define SETM0(x) setm0(x)
#define DMA(voff,rsrc,soff,imm) dma<imm>(voff,rsrc,soff)
#define DMAS(voff,rsrc,soff,imm) dmas<imm>(voff,rsrc,soff)
#define LD4(dst,voff,rsrc,soff,imm) ld4<imm>(dst,voff,rsrc,soff)
#define LD4S(dst,voff,rsrc,soff,imm) ld4s<imm>(dst,voff,rsrc,soff)
#define LD4SA(dst,voff,rsrc,soff,imm) ld4sa<imm>(dst,voff,rsrc,soff)
#define LD2S(dst,voff,rsrc,soff,imm) ld2s<imm>(dst,voff,rsrc,soff)
#define LD2(dst,voff,rsrc,soff,imm) ld2<imm>(dst,voff,rsrc,soff)
#define LD1S(dst,voff,rsrc,soff,imm) ld1s<imm>(dst,voff,rsrc,soff)
#define LD1(dst,voff,rsrc,soff,imm) ld1<imm>(dst,voff,rsrc,soff)
#define ST4S(src,voff,rsrc,soff,imm) st4s<imm>(src,voff,rsrc,soff)
#define ST2S(src,voff,rsrc,soff,imm) st2s<imm>(src,voff,rsrc,soff)
template<int N> DI void waitv(){ asm volatile("s_waitcnt vmcnt(%0)"::"n"(N):"memory"); }
template<class T> DI void pin(T& a){ asm volatile("":"+v"(a)::"memory"); }
template<class T,class... R> DI void pin(T& a,R&... r){ pin(a); pin(r...); }
template<int N,class... R> DI void waitv(R&... r){ asm volatile("s_waitcnt vmcnt(%0)"::"n"(N):"memory"); pin(r...); }
#define WAITV(n) waitv<n>()
#define WAITL() asm volatile("s_waitcnt lgkmcnt(0)":::"memory")
#define BARRIER() asm volatile("s_waitcnt lgkmcnt(0)\n\ts_barrier":::"memory")
DI void MFMA16  (f32x4& acc,const u32x4& a,const u32x4& b){ asm volatile("v_mfma_f32_16x16x32_bf16 %0, %1, %2, %0":"+v"(acc):"v"(a),"v"(b)); }
DI void MFMA16Z (f32x4& acc,const u32x4& a,const u32x4& b){ asm volatile("v_mfma_f32_16x16x32_bf16 %0, %1, %2, 0":"=&v"(acc):"v"(a),"v"(b)); }
DI void MFMA16A (f32x4& acc,const u32x4& a,const u32x4& b){ asm volatile("v_mfma_f32_16x16x32_bf16 %0, %1, %2, %0":"+v"(acc):"a"(a),"v"(b)); }
DI void MFMA16AZ(f32x4& acc,const u32x4& a,const u32x4& b){ asm volatile("v_mfma_f32_16x16x32_bf16 %0, %1, %2, 0":"=&v"(acc):"a"(a),"v"(b)); }
DI void NOP8(){ asm volatile("s_nop 7":::"memory"); __builtin_amdgcn_sched_barrier(0); }
DI float bflo(unsigned x){ return __uint_as_float(x<<16); }
DI float bfhi(unsigned x){ return __uint_as_float(x&0xffff0000u); }
DI unsigned cvtpk(float lo,float hi){ f32x2 v={lo,hi}; bf16x2 b=__builtin_convertvector(v,bf16x2); return __builtin_bit_cast(unsigned,b); }
DI float rbf(float x){ return bflo(cvtpk(x,x)); }
DI u32x4 cat(s16x4 a, s16x4 b){ s16x8 r=__builtin_shufflevector(a,b,0,1,2,3,4,5,6,7); return __builtin_bit_cast(u32x4,r); }
DI u32x4 cat2(u32x2 a,u32x2 b){ return __builtin_shufflevector(a,b,0,1,2,3); }
DI s16x4 trread(unsigned a){ return __builtin_amdgcn_ds_read_tr16_b64_v4i16((LAS s16x4*)(size_t)a); }
DI u32x4 lr128(unsigned a){ return *(const LAS u32x4*)(size_t)a; }
DI f32x4 lr128f(unsigned a){ return *(const LAS f32x4*)(size_t)a; }
DI u32x2 lr64v(unsigned a){ return *(const volatile LAS u32x2*)(size_t)a; }
DI void lw128(unsigned a,u32x4 v){ *(LAS u32x4*)(size_t)a=v; }
DI void lw128f(unsigned a,f32x4 v){ *(LAS f32x4*)(size_t)a=v; }
DI void lw64(unsigned a,u32x2 v){ *(LAS u32x2*)(size_t)a=v; }
DI unsigned opq(unsigned x){ asm volatile("":"+v"(x)); return x; }
template<int CTRL> DI float dpp_add(float v){ return v+__builtin_bit_cast(float,__builtin_amdgcn_update_dpp(0,__builtin_bit_cast(int,v),CTRL,0xf,0xf,true)); }
DI float sum32(float v){
  v=dpp_add<0x128>(v); v=dpp_add<0x124>(v); v=dpp_add<0x122>(v); v=dpp_add<0x121>(v);
  auto r=__builtin_amdgcn_permlane16_swap(__float_as_uint(v),__float_as_uint(v),false,false);
  return __uint_as_float(r[0])+__uint_as_float(r[1]); }

constexpr unsigned OOB=0xfff00000u;
template<int... I,class F> DI void sfor_impl(std::integer_sequence<int,I...>,F&& f){ (f(std::integral_constant<int,I>{}),...); }
template<int N,class F> DI void sfor(F&& f){ sfor_impl(std::make_integer_sequence<int,N>{},f); }
#define SFOR(N,v) sfor<N>([&](auto v##_) __attribute__((always_inline)) { constexpr int v=v##_;
#define SEND });
#define IC(v) std::integral_constant<int,(v)>{}
constexpr unsigned WS_E=0, WS_FP=256, WS_FQ=WS_FP+2048, WS_FH=WS_FQ+256, WS_FY=WS_FH+8192, WS_FLAGS_END=WS_FY+10240;
constexpr unsigned WS_ST=24576  , WS_CS=WS_ST+16384  , WS_UT=WS_CS+57344, WS_WDF=WS_UT+D_*S_*2, WS_HF=WS_WDF+F_*D_*2, WS_YT=WS_HF+S_*F_*2, WS_END=WS_YT+D_*S_*2;
constexpr unsigned WS_DBG=32u<<20;
static_assert(WS_FLAGS_END<=WS_ST,"");
constexpr int STG=49152;
constexpr int L_KV=2*STG, L_CSD=2*STG+16384;
constexpr int L_TAB=3*STG;
constexpr int CV_HC=4160, CV_H=4*CV_HC, CV_U=4608, CV_CH=CV_H+CV_U;
constexpr int L_CV=STG;
constexpr int L_RED=2*STG;
constexpr int L_Y=L_TAB;
constexpr int LDS_BYTES=163840;

#define STAMP(i)

#define STAMP2(i)
#define STAMPM(i) STAMP(i)
#define STAMP5(i)
#define STAMP4(i)
#define STAMP3(i)
#define STAMPL(i) STAMP(i)
DI void spin4(const u32x4& rws, unsigned off, int nlanes, u64 tag){
  const int lane=threadIdx.x&63; const unsigned vo=(off+lane*32)|(lane<nlanes?0:OOB); const unsigned tl=(unsigned)tag, th=(unsigned)(tag>>32);
  const u64 t0=__builtin_amdgcn_s_memrealtime();
  for(;;){
    u32x4 a,c; ld4s<0>(a,vo,rws,0); ld4s<16>(c,vo,rws,0); waitv<0>(a,c);
    const bool ok= a[0]==tl&&a[1]==th&&a[2]==tl&&a[3]==th&&c[0]==tl&&c[1]==th&&c[2]==tl&&c[3]==th;
    if(__all(ok || lane>=nlanes)) break;
    if(__builtin_amdgcn_s_memrealtime()-t0>2000000ull) break;
    __builtin_amdgcn_s_sleep(1);
  }
}
DI void put_flag(char* ws, unsigned off, u64 tag){ __hip_atomic_store((GAS u64*)(ws+off),tag,__ATOMIC_RELAXED,__HIP_MEMORY_SCOPE_AGENT); }
DI bool tag4(u32x4 v,u64 tag){ const unsigned tl=(unsigned)tag, th=(unsigned)(tag>>32); return v[0]==tl&&v[1]==th&&v[2]==tl&&v[3]==th; }

__global__ __launch_bounds__(256,1) void fused_kernel(const u16* __restrict__ x,const u16* __restrict__ kk,const u16* __restrict__ vv,const u16* __restrict__ sk,
    const u16* __restrict__ hk,const u16* __restrict__ Dv,const u16* __restrict__ wg,const u16* __restrict__ wu,const u16* __restrict__ wd,u16* __restrict__ out,char* ws){
  extern __shared__ __attribute__((aligned(16))) char smem[];
  STAMP(0);
  asm volatile("":::"a0","a255");
  asm volatile(""::"s"(x),"s"(kk),"s"(vv),"s"(sk),"s"(hk),"s"(Dv),"s"(wg),"s"(wu),"s"(wd),"s"(out),"s"(ws));
  const int tid=threadIdx.x, lane=tid&63; const unsigned w=rfl(tid>>6); const int b=blockIdx.x;
  const unsigned lds0=(unsigned)(size_t)(LAS char*)smem;
  const u32x4 rws=mk_rsrc(ws,WS_END), rx=mk_rsrc(x,S_*D_*2);
  u32x2 Eraw; { const unsigned z=0; LD2S(Eraw,z,rws,0,0); }
  const int lrow=8*b+2*w+(lane>>5), l32=lane&31;
  u32x4 xc3[3];
  { const unsigned vo=lrow*1280+l32*16; ld4p<NTX,0>(xc3[0],vo,rx,0); ld4p<NTX,512>(xc3[1],vo,rx,0); const unsigned v2=(vo+1024)|(l32<16?0:OOB); ld4p<NTX,0>(xc3[2],v2,rx,0); }
  STAMP3(6);
  constexpr int NPF1=0;
  constexpr int NPF=NPF1;
  auto pinpf=[&]() __attribute__((always_inline)) {
  };
  const int xcd=b&7, jj=b>>3; const bool isg=jj<28;
  const int g1id=xcd*28+jj, spid=xcd*4+jj-28;
  const int mi=(xcd>>1)*4+(jj&3), ni=(xcd&1)*7+(jj>>2);
  const int gj=lane&15,gq=lane>>4;
  const bool iskv=isg && g1id>=64; const int kvt=g1id-64;
  const int tb=kvt/5, cb=kvt-5*tb, t0=tb*64, pc_=tid&15, tg=tid>>4, d0=cb*128+pc_*8;
  STAMP3(7);
  const u32x4 rg=mk_rsrc(wg,D_*F_*2), ru=mk_rsrc(wu,D_*F_*2);
  unsigned vx[4], vw[4];
  #pragma unroll
  for(int i=0;i<4;i++){ const int row=32*w+8*i+(lane>>3); const int c=(lane&7)^((row>>1)&7); vx[i]=(128*mi+row)*1280+c*16-1024*i;
    const int k=16*w+4*i+(lane>>4), cp=lane&15, gs=(cp>>1)^((k&3)|(((k>>3)&1)<<2)); vw[i]=k*3456+ni*256+gs*32+(cp&1)*16-1024*i; }
  const unsigned m0x=rfl(lds0+w*4096), m0g=m0x+16384, m0u=m0x+32768;
  auto piece=[&](int P,int st,int bf) __attribute__((always_inline)) {
    if(P==0) SETM0(m0x+bf*STG); if(P==4) SETM0(m0g+bf*STG); if(P==8) SETM0(m0u+bf*STG);
    const unsigned sx=st*128, sw=st*221184;
    if(P==0) dmap<NTSX,0>(vx[0],rx,sx); if(P==1) dmap<NTSX,1024>(vx[1],rx,sx); if(P==2) dmap<NTSX,2048>(vx[2],rx,sx); if(P==3) dmap<NTSX,3072>(vx[3],rx,sx);
    if(P==4) dmap<NTSW,0>(vw[0],rg,sw); if(P==5) dmap<NTSW,1024>(vw[1],rg,sw); if(P==6) dmap<NTSW,2048>(vw[2],rg,sw); if(P==7) dmap<NTSW,3072>(vw[3],rg,sw);
    if(P==8) dmap<NTSW,0>(vw[0],ru,sw); if(P==9) dmap<NTSW,1024>(vw[1],ru,sw); if(P==10) dmap<NTSW,2048>(vw[2],ru,sw); if(P==11) dmap<NTSW,3072>(vw[3],ru,sw);
  };
  u64 tag;
  auto stats=[&]() __attribute__((always_inline)) {
  tag=((u64)rfl(Eraw[0])|((u64)rfl(Eraw[1])<<32))*6364136223846793005ull+1442695040888963407ull;
  {
    f32x2 f[12];
    #pragma unroll
    for(int i=0;i<3;i++) for(int e=0;e<4;e++) f[4*i+e]=f32x2{bflo(xc3[i][e]),bfhi(xc3[i][e])};
    f32x2 s0=(f[0]+f[1])+(f[2]+f[3]), s1=(f[4]+f[5])+(f[6]+f[7]), s2=(f[8]+f[9])+(f[10]+f[11]); s0=(s0+s1)+s2;
    const float mean=sum32(s0[0]+s0[1])*(1.f/640.f);
    f32x2 q0={0.f,0.f},q1={0.f,0.f},q2={0.f,0.f};
    #pragma unroll
    for(int i=0;i<4;i++){ f32x2 d=f[i]-mean, e=f[4+i]-mean, g=f[8+i]-mean; q0=d*d+q0; q1=e*e+q1; q2=g*g+q2; }
    q0+=q1;
    const float var=rbf(sum32(q0[0]+q0[1]+(l32<16? q2[0]+q2[1]:0.f))*(1.f/640.f)); const float m=rbf(mean);
    const float den=rbf(__builtin_amdgcn_sqrtf(rbf(var+1e-5f)));
    u32x2 sv={__float_as_uint(m),__float_as_uint(__builtin_amdgcn_rcpf(den))}; const unsigned vo=(WS_ST+lrow*8)|(l32==0?0:OOB);
    ST2S(sv,vo,rws,0,0);
  }
  };
  const int mt=(xcd>>1)*8+(jj&7), nt=(xcd&1)*4+(jj>>3);
  const u32x4 rhk=mk_rsrc(hk,D_*S_*2);
  const int cvp=w>>1, cvh=w&1;
  const unsigned ldsC=rfl(lds0+L_CV+cvp*CV_CH);
  const int chl=128+2*b+cvp;
  const unsigned vw0=lane*16, vw1=vw0+4096;
  const int kst=14*w-(w==3?1:0), ns= w<2? 14:13, rot=ROT*(((jj>>3)*ns)>>2);
  unsigned k1k[14];
  #pragma unroll
  for(int i=0;i<14;i++){ int t=i+rot; t= t>=ns? t-ns: t; k1k[i]=rfl((kst+t)*1024); }
  const unsigned wdbase=rfl(WS_WDF+nt*54*5*1024);
  auto ldwd=[&](auto NC) __attribute__((always_inline)) { constexpr int n=decltype(NC)::value, i=n/5, dt=n%5;
    const unsigned so=rfl(wdbase+5*k1k[i]); if constexpr(dt<4) ldwa<n,dt*1024>(vw0,rws,so); else ldwa<n,0>(vw1,rws,so); };
  auto conv_issue=[&](unsigned base,int ch,int part) __attribute__((always_inline)) {
    const unsigned so=rfl(ch*4096);
    if(part<2){ const int c=2*cvh+part; const unsigned vo=lane*16+2*c; SETM0(base+c*CV_HC+32); dman<0>(vo,rhk,so); dman<1024>(vo,rhk,so); dman<2048>(vo,rhk,so); dman<3072>(vo,rhk,so); }
    else { const unsigned vo=WS_UT+lane*16+cvh*2048; SETM0(base+CV_H+cvh*2048); DMAS(vo,rws,so,0); DMAS(vo,rws,so,1024); }
  };
  const unsigned ldsR0=rfl(lds0+cvp*CV_CH), ldsR1=rfl(lds0+2*STG+16384+cvp*CV_CH);
  constexpr int NWE=44;
  if(isg){
    auto middle=[&](auto NLC, unsigned& tk) __attribute__((always_inline)) { constexpr int NL=decltype(NLC)::value;
      { const unsigned vo=((128+2*b)*4096+lane*128)|((w==0&&TKM)?0:OOB); if constexpr(TKM==2) ld1n<0>(tk,vo,rhk,0); else LD1(tk,vo,rhk,0,0); }
      waitv<NL+1+NPF>(Eraw,xc3[0],xc3[1],xc3[2]);
      STAMP3(8);
      stats();
      STAMPM(1);
      #pragma unroll
      for(int P=0;P<12;P++) piece(P,0,0); };
    const unsigned P=lds0+L_KV;
    if(iskv){
      u32x4 pr[15];
      const u32x4 rk=mk_rsrc(kk,S_*D_*2), rv=mk_rsrc(vv,S_*D_*2), rsk=mk_rsrc(sk,D_*8);
      const unsigned vb=((t0+4*tg)*D_+d0)*2, va=(vb-3840)|((t0+tg==0)?OOB:0), vs=d0*8;
      ld4n<0>(pr[0],va,rk,0); ld4n<1280>(pr[1],va,rk,0); ld4n<2560>(pr[2],va,rk,0);
      ld4n<0>(pr[3],vb,rk,0); ld4n<1280>(pr[4],vb,rk,0); ld4n<2560>(pr[5],vb,rk,0); ld4n<3840>(pr[6],vb,rk,0);
      ld4n<0>(pr[7],vb,rv,0); ld4n<1280>(pr[8],vb,rv,0); ld4n<2560>(pr[9],vb,rv,0); ld4n<3840>(pr[10],vb,rv,0);
      ld4<0>(pr[11],vs,rsk,0); ld4<16>(pr[12],vs,rsk,0); ld4<32>(pr[13],vs,rsk,0); ld4<48>(pr[14],vs,rsk,0);
      unsigned vu=opq(WS_UT+(cb*128+(tid>>3))*4096+(S_-64-t0+(tid&7)*8)*2), lw_=opq(P+8*pc_*128+((15-tg)^pc_)*8);
      unsigned tk; middle(IC(15),tk);
      waitv<13>(pr[0],pr[1],pr[2],pr[3],pr[4],pr[5],pr[6],pr[7],pr[8],pr[9],pr[10],pr[11],pr[12],pr[13],pr[14],tk);
      pinpf();
      STAMP3(9);
      #pragma unroll
      for(int e2=0;e2<4;e2++){
        piece(3*e2,1,1); piece(3*e2+1,1,1); piece(3*e2+2,1,1);
        const f32x2 w0={bflo(pr[11+e2][0]),bflo(pr[11+e2][2])}, w1={bfhi(pr[11+e2][0]),bfhi(pr[11+e2][2])}, w2={bflo(pr[11+e2][1]),bflo(pr[11+e2][3])}, w3={bfhi(pr[11+e2][1]),bfhi(pr[11+e2][3])};
        f32x2 kf[7];
        #pragma unroll
        for(int i=0;i<7;i++) kf[i]=f32x2{bflo(pr[i][e2]),bfhi(pr[i][e2])};
        f32x2 o[4];
        #pragma unroll
        for(int i=0;i<4;i++){ f32x2 ks=kf[i]*w3; ks=kf[i+1]*w2+ks; ks=kf[i+2]*w1+ks; ks=kf[i+3]*w0+ks; o[i]=ks*f32x2{bflo(pr[7+i][e2]),bfhi(pr[7+i][e2])}; }
        #pragma unroll
        for(int z=0;z<2;z++){ u32x2 pk={cvtpk(o[3][z],o[2][z]),cvtpk(o[1][z],o[0][z])}; lw64(lw_+(2*e2+z)*128,pk); }
      }
      STAMP3(10);
      BARRIER();
      STAMP3(11);
      u32x4 v[4];
      #pragma unroll
      for(int i=0;i<4;i++) v[i]=lr128(P+((tid>>3)+32*i)*128+(((tid&7)^((w>>1)+2*i))*16));
      #pragma unroll
      for(int i=0;i<4;i++){ const u32x4 o= (w&1)? u32x4{v[i][2],v[i][3],v[i][0],v[i][1]} : v[i]; st4s<0>(o,vu,rws,i*131072); }
    } else {
      u32x4 pr[12];
      const u32x4 rwd=mk_rsrc(wd,F_*D_*2);
      const unsigned vo=(lane>>3)*1280+(lane&7)*16; unsigned so[3], sd[3]; int c4[3];
      #pragma unroll
      for(int z=0;z<3;z++){ const int un=4*g1id+w+256*z, s=un/10, c64=un-10*s; so[z]=rfl(un<540? (32*s*D_+c64*64)*2 : 0x10000000); sd[z]=rfl(un<540? WS_WDF+s*5120 : 0x10000000); c4[z]=4*c64; }
      SFOR(12,i) ld4n<0>(pr[i],vo,rwd,so[i>>2]+(i&3)*8*1280); SEND
      unsigned tk; middle(IC(12),tk);
      waitv<13>(pr[0],pr[1],pr[2],pr[3],pr[4],pr[5],pr[6],pr[7],pr[8],pr[9],pr[10],pr[11],tk);
      pinpf();
      const unsigned L=P+w*12288;
      #pragma unroll
      for(int z=0;z<3;z++){
        #pragma unroll
        for(int i=0;i<4;i++){ piece(4*z+i,1,1); const int r=8*i+(lane>>3); lw128(L+z*4096+r*128+(((lane&7)^((r>>2)&7))*16),pr[4*z+i]); } }
      BARRIER();
      const int qq=(lane&15)>>2, p=lane&3, r=4*gq+qq;
      SFOR(12,i) constexpr int z=i>>2, f4=i&3; const int ch=2*f4+(p>>1);
        const u32x4 v=cat(trread(L+z*4096+r*128+((ch^((r>>2)&7))*16)+(p&1)*8),trread(L+z*4096+(r+16)*128+((ch^(((r+16)>>2)&7))*16)+(p&1)*8));
        const int D16=c4[z]+f4, n5=D16/5, dt=D16-5*n5; st4s<0>(v,vw0,rws,rfl(sd[z]+n5*276480+dt*1024)); SEND
    }
    STAMPM(2);
    const int wm=w>>1, wn=w&1;
    unsigned xa[2], wa[4];
    { const int qq=(lane&15)>>2, p=lane&3;
      #pragma unroll
      for(int j=0;j<2;j++) xa[j]=opq(lds0+(64*wm+gj)*128+(((4*j+gq)^((lane>>1)&7))*16));
      #pragma unroll
      for(int z=0;z<4;z++) wa[z]=opq(lds0+16384+(8*gq+qq)*256+(((4*wn+z)^(qq|((gq&1)<<2)))*32)+p*8); }
    f32x4 ag[4][4], au[4][4];
    u32x4 fw[4], fx[2][4];
    u32x4 fp0,fp1; u32x2 fq, st[4]; unsigned csp[2];
    auto rdw=[&](auto GC,auto JC,auto ZC,auto HC) __attribute__((always_inline)) {
      constexpr int G=decltype(GC)::value, J=decltype(JC)::value, Z=decltype(ZC)::value, Hh=decltype(HC)::value;
      const u32x2 t=__builtin_bit_cast(u32x2,trread(wa[Z&3]+J*8192+(Z>>2)*16384+Hh*1024));
      if constexpr(Hh==0){ u32x4 v={t[0],t[1],t[0],t[1]}; fw[G&3]=v; } else { fw[G&3][2]=t[0]; fw[G&3][3]=t[1]; } };
        WAITV(16); BARRIER();
    #pragma unroll
    for(int m=0;m<4;m++) fx[0][m]=lr128(xa[0]+m*2048);
    SFOR(3,z) rdw(IC(z),IC(0),IC(z),IC(0)); rdw(IC(z),IC(0),IC(z),IC(1)); SEND
    STAMPM(3); STAMP5(1);
    SFOR(10,s)
      SFOR(2,j)
        constexpr int c=j&1,n=c^1;
        SFOR(8,z)
          constexpr int G=(2*s+j)*8+z;
          SFOR(4,m)
            f32x4& A= z<4? ag[z&3][m] : au[z&3][m];
            if constexpr(s==0&&j==0) MFMA16Z(A,fw[G&3],fx[c][m]); else MFMA16(A,fw[G&3],fx[c][m]);
            if constexpr(j==0){
              if constexpr(m<2){ if constexpr(z<5) rdw(IC(G+3),IC(0),IC(z+3),IC(m)); else rdw(IC(G+3),IC(1),IC(z-5),IC(m)); }
              if constexpr(m==2 && z>=4) fx[n][z-4]=lr128(xa[1]+(z-4)*2048);
            } else {
              if constexpr(z<5 && m<2) rdw(IC(G+3),IC(1),IC(z+3),IC(m));
              if constexpr(s<9 && z==6){ if constexpr(m<2){ rdw(IC(G+2),IC(0),IC(0),IC(m)); fx[n][m]=lr128(xa[0]+m*2048); } else rdw(IC(G+3),IC(0),IC(1),IC(m-2)); }
              if constexpr(s<9 && z==7 && m<2){ rdw(IC(G+3),IC(0),IC(2),IC(m)); fx[n][2+m]=lr128(xa[0]+(2+m)*2048); }
            }
            if constexpr(s==0&&j==0){ if constexpr(z<6 && (m&1)) piece(2*z+(m>>1),2,2); }
            else if constexpr(m==3){
              if constexpr(j==1 && z>=6 && s+3<10) piece(z-6,s+3,(s+3)%3);
              if constexpr(j==0 && s+2<10) piece(2+z,s+2,(s+2)%3);
              if constexpr(j==1 && z<2 && s>=1 && s+2<10) piece(10+z,s+2,(s+2)%3);
              if constexpr(s==7 && j==1 && z==2){ const unsigned vo=WS_FP+lane*32; LD4S(fp0,vo,rws,0,0); LD4S(fp1,vo,rws,0,16);
                const unsigned vq=(WS_FQ+lane*8)|(lane<32?0:OOB); LD2S(fq,vq,rws,0,0); }
              if constexpr(s==8 && j==0 && (z==1||z==5)) conv_issue(ldsC,chl,z>>2);
            }
            GAP();
          SEND
          if constexpr(j==1 && z==5 && s<9){
            if constexpr(s==0) WAITV(16); else if constexpr(s<=6) WAITV(12); else if constexpr(s==7) WAITV(15); else WAITV(8);
            if constexpr(s==8){
              pin(fp0,fp1,fq);
              if(!__all(tag4(fp0,tag)&&tag4(fp1,tag)&&((fq[0]==(unsigned)tag&&fq[1]==(unsigned)(tag>>32))||lane>=32))){
                spin4(rws,WS_FP,64,tag); spin4(rws,WS_FQ,8,tag); }
              if(b==0 && tid==0) put_flag(ws,WS_E,tag);
              #pragma unroll
              for(int m=0;m<4;m++){ const unsigned vo=WS_ST+(128*mi+64*wm+16*m+gj)*8; LD2S(st[m],vo,rws,0,0); }
              const int col= tid<128? ni*128+tid : 1728+ni*128+tid-128; const unsigned vo=(WS_CS+col*4)|(((tid&127)+ni*128<1728)?0:OOB);
              LD1S(csp[0],vo,rws,0,0); { const unsigned so=13824; LD1S(csp[1],vo,rws,so,0); }
              conv_issue(ldsC,chl,2);
            }
            BARRIER();
            STAMP5(s+2);
            if constexpr(s==1) if(tid==0) put_flag(ws,WS_FP+b*8,tag);
            constexpr int dl= (s+1)%3==0? -2*STG : STG;
            xa[0]=opq(xa[0]+dl); xa[1]=opq(xa[1]+dl); wa[0]=opq(wa[0]+dl); wa[1]=opq(wa[1]+dl); wa[2]=opq(wa[2]+dl); wa[3]=opq(wa[3]+dl);
            GAP();
          }
        SEND
      SEND
    SEND
    STAMPM(4); STAMP5(11);
    waitv<2>(csp[0],csp[1],st[0],st[1],st[2],st[3]);
    *(LAS float*)(size_t)(lds0+L_TAB+tid*4)=__uint_as_float(csp[0])+__uint_as_float(csp[1]);
    BARRIER();
    NOP8();
    { const float L2E=1.4426950408889634f;
      f32x4 cg[4], cu[4];
      #pragma unroll
      for(int z=0;z<4;z++){ cg[z]=lr128f(lds0+L_TAB+(64*wn+16*z+4*gq)*4); cu[z]=lr128f(lds0+L_TAB+512+(64*wn+16*z+4*gq)*4); }
      SFOR(4,i) ldwd(IC(i)); SEND
      SFOR(4,m)
        const float mean=__uint_as_float(st[m][0]), r=__uint_as_float(st[m][1]);
        const float R=-L2E*r, C=-r*(1.f/L2E), nm=-mean;
        SFOR(2,G)
          u32x4 o;
          SFOR(4,te)
            constexpr int T=te>>1, e=te&1, z=2*G+T;
            f32x2 g={ag[z][m][2*e],ag[z][m][2*e+1]}, u={au[z][m][2*e],au[z][m][2*e+1]};
            g=f32x2{cg[z][2*e],cg[z][2*e+1]}*nm+g; u=f32x2{cu[z][2*e],cu[z][2*e+1]}*nm+u;
            f32x2 s=g*R; f32x2 ex={__builtin_amdgcn_exp2f(s[0]),__builtin_amdgcn_exp2f(s[1])};
            f32x2 d=ex+1.f; f32x2 rc={__builtin_amdgcn_rcpf(d[0]),__builtin_amdgcn_rcpf(d[1])};
            f32x2 h=((u*C)*s)*rc; o[2*T+e]=cvtpk(h[0],h[1]);
            ldwd(IC(4+(m*2+G)*5+te));
            GAP();
          SEND
          const int s32=ni*4+2*wn+G, m16=mi*8+wm*4+m;
          const unsigned vo=(WS_HF+((m16*54+s32)*64+lane)*16)|(s32<54?0:OOB); ST4S(o,vo,rws,0,0);
          ldwd(IC(4+(m*2+G)*5+4));
        SEND
      SEND }
    STAMPM(5);
    WAITV(52);
  } else {
    unsigned tk0;
    {
      const int gw=4*spid+w, cbk=gw>>1, kh=gw&1; const bool act=gw<108;
      const u32x4 rcs=mk_rsrc(cbk<27? wg:wu, D_*F_*2); const int cb64=cbk<27? cbk: cbk-27;
      unsigned vc[2];
      #pragma unroll
      for(int i=0;i<2;i++){ const int r=8*i+(lane>>3), cp=lane&7; const int gs=(cp>>1)^(((r>>1)&1)|((i&1)<<1)); vc[i]=((320*kh+r)*3456+cb64*128+gs*32+(cp&1)*16)|(act?0:OOB); }
      const unsigned LB=rfl(lds0+w*16384);
      auto csissue=[&](int c) __attribute__((always_inline)) {
        #pragma unroll
        for(int i=0;i<8;i++){ SETM0(LB+(c&1)*8192+i*1024); const unsigned so=(64*c+16*(i>>1))*3456; dmap<NTCS,0>(vc[i&1],rcs,so); } };
      { const unsigned vo=((w<2? 4*spid+2*w : 128+2*b)*4096+lane*128)|((w<3&&TKM)?0:OOB); if constexpr(TKM==2) ld1n<0>(tk0,vo,rhk,0); else LD1(tk0,vo,rhk,0,0); }
      csissue(0); csissue(1);
      waitv<17+NPF>(Eraw,xc3[0],xc3[1],xc3[2]);
      stats();
      STAMPM(1);
      WAITV(0); pinpf();
      BARRIER();
      if(tid==0) put_flag(ws,WS_FP+b*8,tag);
      const int qq=(lane&15)>>2, p=lane&3; f32x4 cs[4]; u32x4 on={0x3f803f80u,0x3f803f80u,0x3f803f80u,0x3f803f80u}; asm volatile("s_nop 1":"+v"(on));
      SFOR(5,c)
        if constexpr(c>=1 && c<4) WAITV(8); else if constexpr(c==4) WAITV(0);
        u32x4 fa[2][4];
        SFOR(2,k) SFOR(4,z) const int r0=32*k+8*gq+qq; const int g0=((r0>>1)&1)|(((r0>>3)&1)<<1);
          fa[k][z]=cat(trread(LB+(c&1)*8192+r0*128+((z^g0)*32)+p*8),trread(LB+(c&1)*8192+(r0+4)*128+((z^g0)*32)+p*8)); SEND SEND
        WAITL();
        if constexpr(c+2<5) csissue(c+2);
        GAP();
        SFOR(2,k) SFOR(4,z) if constexpr(c==0&&k==0) MFMA16Z(cs[z],fa[k][z],on); else MFMA16(cs[z],fa[k][z],on); SEND SEND
        GAP();
      SEND
      NOP8(); pin(cs[0],cs[1],cs[2],cs[3]);
      f32x4 v;
      #pragma unroll
      for(int e=0;e<4;e++) v[e]= gj==0? cs[0][e] : gj==1? cs[1][e] : gj==2? cs[2][e] : cs[3][e];
      const unsigned vo=(WS_CS+(kh*3456+cbk*64+16*gj+4*gq)*4)|((act&&gj<4)?0:OOB); ST4S(__builtin_bit_cast(u32x4,v),vo,rws,0,0);
    }
    STAMPM(3);
    BARRIER();
    conv_issue(ldsR0,4*spid+cvp,0); conv_issue(ldsR0,4*spid+cvp,1);
    conv_issue(ldsR1,4*spid+2+cvp,0); conv_issue(ldsR1,4*spid+2+cvp,1);
    conv_issue(ldsC,chl,0); conv_issue(ldsC,chl,1);
    waitv<24>(tk0); BARRIER();
    if(tid==0) put_flag(ws,WS_FQ+spid*8,tag);
    STAMPM(4);
    STAMPM(5);
  }
  u32x2 ph, xv[5], stv;
  const int trow=64*mt+16*w+gj;
  u32x4 H[14][4];
  unsigned vh[4];
  #pragma unroll
  for(int r=0;r<4;r++) vh[r]=WS_HF+((w+r)&3)*54*1024+lane*16;
  auto hoff=[&](int i) __attribute__((always_inline)) ->unsigned { return rfl((i<13||w<2)? (unsigned)(4*mt*54*1024)+k1k[i] : 0x10000000u); };
  auto ldH=[&](int i) __attribute__((always_inline)) {
    const unsigned sh=rfl((i<13||w<2)? (unsigned)(4*mt*54*1024)+k1k[i] : 0x10000000u);

    LD4S(H[i][0],vh[0],rws,sh,0); LD4S(H[i][1],vh[1],rws,sh,0); LD4S(H[i][2],vh[2],rws,sh,0); LD4S(H[i][3],vh[3],rws,sh,0); };
  auto conv_round=[&](auto LASTC, const unsigned base, const int ch) __attribute__((always_inline)) {
    constexpr bool LAST=decltype(LASTC)::value;
    BARRIER();
    if(cvh==0){
      const int c=lane>>4,p=lane&15; const LAS u16* src=(const LAS u16*)(size_t)(base+32+ (p>=16-c? (p-16+c)*2:0));
      u16 v=*src; *(LAS u16*)(size_t)(base+c*CV_HC+p*2)= p>=16-c? v:(u16)0;
    } else if(lane<32) lw128(base+CV_H+4096+lane*16,u32x4{0,0,0,0});
    const float Dd=bflo(((const unsigned*)Dv)[ch>>1]>>((ch&1)*16));
    BARRIER();
    if constexpr(LAST){ STAMPL(6); }
    const int ci=gj,cq=gq,cc=(ci+1)&3;
    const unsigned pa=opq(base+cc*CV_HC+2*(1+ci-cc+8*cq)+256*cvh), pb=opq(base+CV_H+2*(2032-16*ci+8*cq)-3584+256*cvh);
    f32x4 ya[8];
    { u32x4 B[2][8], A[2][8];
      auto ldA=[&](int rho,int a)->u32x4{ const unsigned p=pa+64*rho+512*a; return cat2(lr64v(p),lr64v(p+8)); };
      auto ldB=[&](int rho,int bb)->u32x4{ return lr128(pb+64*rho+512*(7-bb)); };
      #pragma unroll
      for(int z=0;z<8;z++){ B[0][z]=ldB(0,z); A[0][z]=ldA(0,z); }
      GAP();
      SFOR(4,rho)
        constexpr int c=rho&1,n=c^1;
        if constexpr(LAST && rho==2){
          WAITV(15); if(isg && lane==0) put_flag(ws,WS_FH+(mi*64+ni*4+w)*8,tag); }
        if constexpr(LAST && rho==3){ const unsigned vo=(WS_FH+((mt>>1)*64+lane)*8)|(lane<56?0:OOB); LD2S(ph,vo,rws,0,0); }
        SFOR(8,a)
          SFOR(8-a,nn_)
            constexpr int nu=a+nn_; constexpr int cnt=a*8-(a*(a-1))/2+(nu-a);
            if constexpr(rho==0 && a==0) MFMA16Z(ya[nu],A[c][a],B[c][nu-a]); else MFMA16(ya[nu],A[c][a],B[c][nu-a]);
            if constexpr(rho<3){
              if constexpr(cnt<8) B[n][cnt]=ldB(rho+1,cnt);
              else if constexpr(cnt<24){ constexpr int az=(cnt-8)>>1; const u32x2 t=lr64v(pa+64*(rho+1)+512*az+8*(cnt&1));
                if constexpr((cnt&1)==0){ u32x4 v={t[0],t[1],t[0],t[1]}; A[n][az]=v; } else { A[n][az][2]=t[0]; A[n][az][3]=t[1]; } } }
            if constexpr(LAST && rho<3 && cnt>=24 && cnt<31){ constexpr int nn=NWE+rho*7+(cnt-24); if constexpr(nn<64) ldwd(IC(nn)); }
            if constexpr(LAST && rho==3 && cnt>=4 && cnt<28 && cnt%4==0){
              constexpr int q=cnt/4-1; const unsigned vo=(trow*D_+80*nt+4*gq)*2;
              if constexpr(q<5) ld2<32*q>(xv[q],vo,rx,0); else { const unsigned vs=WS_ST+trow*8; LD2S(stv,vs,rws,0,0); } }
            GAP();
          SEND
        SEND
      SEND
    }
    if constexpr(LAST){ STAMPL(7);
      waitv<6>(ph);
      if(!__all((ph[0]==(unsigned)tag && ph[1]==(unsigned)(tag>>32)) || lane>=56)) spin4(rws,WS_FH+(mt>>1)*512,14,tag);
      ldH(0); ldH(1); ldH(2); ldH(3); }
    NOP8();
    f32x4 mine[4];
    { const unsigned R=lds0+L_RED+w*4096+lane*16;
      #pragma unroll
      for(int z=0;z<4;z++){ const f32x4 lo=ya[z], hi=ya[4+z]; f32x4 snd, kp;
        #pragma unroll
        for(int e=0;e<4;e++){ snd[e]= cvh? lo[e]:hi[e]; kp[e]= cvh? hi[e]:lo[e]; }
        lw128f(R+z*1024,snd); mine[z]=kp; } }
    BARRIER();
    { const unsigned R=lds0+L_RED+(w^1)*4096+lane*16; unsigned pk[4][2];
      #pragma unroll
      for(int z=0;z<4;z++){ const f32x4 y=mine[z]+lr128f(R+z*1024);
        const int t=256*(4*cvh+z)+16*ci+4*cq;
        const u32x2 uu=*(const LAS u32x2*)(size_t)(base+CV_H+2*(2044-t));
        pk[z][0]=cvtpk(fmaf(bfhi(uu[1]),Dd,y[0]),fmaf(bflo(uu[1]),Dd,y[1])); pk[z][1]=cvtpk(fmaf(bfhi(uu[0]),Dd,y[2]),fmaf(bflo(uu[0]),Dd,y[3])); }
      #pragma unroll
      for(int z=0;z<4;z+=2){
        auto r0=__builtin_amdgcn_permlane16_swap(pk[z][0],pk[z+1][0],false,false);
        auto r1=__builtin_amdgcn_permlane16_swap(pk[z][1],pk[z+1][1],false,false);
        u32x4 v={r0[0],r1[0],r0[1],r1[1]};
        const int t=256*(4*cvh+z+(cq&1))+16*ci+8*(cq>>1);
        const unsigned vo=WS_YT+ch*4096+t*2; ST4S(v,vo,rws,0,0); }
    }
  };
  if(!isg){
    spin4(rws,WS_FP,64,tag);
    conv_issue(ldsR0,4*spid+cvp,2); conv_issue(ldsR1,4*spid+2+cvp,2); conv_issue(ldsC,chl,2);
    unsigned tw[9];
    SFOR(9,i) const unsigned li=((jj-28)*4+w)*540+i*64+lane; const unsigned vt=(WS_WDF+(xcd&1)*4*276480+li*128)|((i*64+lane<540)?0:OOB); ld1s<0>(tw[i],vt,rws,0); SEND
    WAITV(13);
    STAMP4(6);
    conv_round(std::integral_constant<bool,false>{},ldsR0,4*spid+cvp);
    STAMP4(7);
    WAITV(13);
    conv_round(std::integral_constant<bool,false>{},ldsR1,4*spid+2+cvp);
    STAMP4(8);
    waitv<2>(tw[0],tw[1],tw[2],tw[3],tw[4],tw[5],tw[6],tw[7],tw[8]);
    if(lane==0) put_flag(ws,WS_FY+((4*spid+cvp)*2+cvh)*8,tag);
    spin4(rws,WS_FQ,8,tag);
    if(lane==0) put_flag(ws,WS_FY+((4*spid+2+cvp)*2+cvh)*8,tag);
    STAMP4(9);
    SFOR(NWE,n) ldwd(IC(n)); SEND
    STAMP4(10);
  }
  conv_round(std::integral_constant<bool,true>{},ldsC,chl);
  STAMPL(8);
  u32x4 WV[6]; u32x4 py0,py1; u32x4 yv[3];
  f32x4 oa[4][5];
  #define HREGS(i) H[i][0],H[i][1],H[i][2],H[i][3]
  SFOR(14,i)
    if constexpr(i==0){ waitv<14>(HREGS(0),xv[0],xv[1],xv[2],xv[3],xv[4],stv); }
    if constexpr(i==1){ waitv<14>(HREGS(1)); STAMP2(1);
                        }
    if constexpr(i==2) waitv<14>(HREGS(2));
    if constexpr(i==3){ waitv<12>(HREGS(3)); STAMP2(2);
                        if(lane==0) put_flag(ws,WS_FY+(chl*2+cvh)*8,tag); }
    if constexpr(i==4) waitv<12>(HREGS(4));
    if constexpr(i==5) waitv<18>(HREGS(5));
    if constexpr(i==6) waitv<20>(HREGS(6));
    if constexpr(i==7){ waitv<20>(HREGS(7)); STAMP2(3); }
    if constexpr(i==8) waitv<14>(HREGS(8),WV[0],WV[1],WV[2],WV[3],WV[4],WV[5]);
    if constexpr(i==9){ waitv<12>(HREGS(9),py0,py1); STAMP2(4);
              if(!__all((tag4(py0,tag)&&tag4(py1,tag))||lane>=40)){
                spin4(rws,WS_FY+160*nt*8,40,tag); } }
    if constexpr(i==10){ waitv<12>(HREGS(10)); STAMP2(5); }
    if constexpr(i==11) waitv<11>(HREGS(11));
    if constexpr(i==12) waitv<7>(HREGS(12));
    if constexpr(i==13) waitv<3>(HREGS(13));
    GAP();
    SFOR(4,tt)
      SFOR(5,dt)
        constexpr int nn=5*i+dt, sl=tt*5+dt;
        if constexpr(i==0) mfmawz<nn>(oa[tt][dt],H[0][tt]);
        else if constexpr(nn<64) mfmaw<nn>(oa[tt][dt],H[i][tt]);
        else MFMA16(oa[tt][dt],WV[nn-64],H[i][tt]);
        if constexpr(i+4<14 && sl%4==1 && sl<16){ const unsigned sh=hoff(i+4); ld4s<0>(H[i+4][sl>>2],vh[sl>>2],rws,sh); }
        if constexpr(i==4 && sl%3==0 && sl>=2 && sl<20){ constexpr int z=sl/3-1; if constexpr(z<6){ constexpr int n2=64+z, i2=n2/5, d2=n2%5;
            const unsigned so=rfl((i2<13||w<2)? wdbase+5*k1k[i2]+d2*1024 : 0x10000000u); ld4s<0>(WV[z],vw0,rws,so); } }
        if constexpr(i==5 && sl==17){ const unsigned vo=(WS_FY+(160*nt+4*lane)*8)|(lane<40?0:OOB); LD4S(py0,vo,rws,0,0); LD4S(py1,vo,rws,0,16); }
        if constexpr(i==10 && sl%6==2){ constexpr int z=sl/6; const int id=tid+256*z; const int r=id>>3,c8=id&7;
            const unsigned vo=(WS_YT+(80*nt+r)*4096+(64*mt+c8*8)*2)|(id<640?0:OOB); LD4S(yv[z],vo,rws,0,0); }
        GAP();
      SEND
    SEND
  SEND
  STAMPL(9);
  waitv<0>(yv[0],yv[1],yv[2]);
  #pragma unroll
  for(int z=0;z<3;z++){ const int id=tid+256*z; if(id<640) lw128(lds0+L_Y+(id>>3)*160+(id&7)*16,yv[z]); }
  BARRIER();
  NOP8();
  #pragma unroll
  for(int r=1;r<4;r++){ const unsigned a=lds0+((((w+r)&3)*3+(r-1))*5)*1024+lane*16;
    #pragma unroll
    for(int dt=0;dt<5;dt++) lw128f(a+dt*1024,oa[r][dt]); }
  BARRIER();
  STAMPL(10);
  { const float m=__uint_as_float(stv[0]), rd=__uint_as_float(stv[1]); const unsigned a=lds0+(w*15)*1024+lane*16;
    #pragma unroll
    for(int dt=0;dt<5;dt++){
      f32x4 f=(oa[0][dt]+lr128f(a+dt*1024))+(lr128f(a+(5+dt)*1024)+lr128f(a+(10+dt)*1024));
      const u32x2 y2=__builtin_bit_cast(u32x2,trread(lds0+L_Y+(16*dt+4*gq+((lane&15)>>2))*160+(16*w+4*(lane&3))*2));
      const float xf[4]={bflo(xv[dt][0]),bfhi(xv[dt][0]),bflo(xv[dt][1]),bfhi(xv[dt][1])};
      const float yf[4]={bflo(y2[0]),bfhi(y2[0]),bflo(y2[1]),bfhi(y2[1])};
      float o[4];
      #pragma unroll
      for(int e=0;e<4;e++) o[e]=fmaf((xf[e]-m)*rd,yf[e],xf[e])+f[e];
      u32x2 ov={cvtpk(o[0],o[1]),cvtpk(o[2],o[3])};
      *(u32x2*)(out+(size_t)trow*D_+80*nt+16*dt+4*gq)=ov;
    } }
  STAMPL(11);
}

extern "C" void kernel_launch(void* const* d_in, const int* in_sizes, int n_in, void* d_out, int out_size, void* d_ws, size_t ws_size, hipStream_t stream){
  (void)hipFuncSetAttribute((const void*)fused_kernel,hipFuncAttributeMaxDynamicSharedMemorySize,LDS_BYTES);
  fused_kernel<<<256,256,LDS_BYTES,stream>>>((const u16*)d_in[0],(const u16*)d_in[1],(const u16*)d_in[2],(const u16*)d_in[3],(const u16*)d_in[4],(const u16*)d_in[5],
      (const u16*)d_in[7],(const u16*)d_in[8],(const u16*)d_in[9],(u16*)d_out,(char*)d_ws);
}
```

```cpp
#define XMODE 2
#define XWG 250
#include <hip/hip_runtime.h>
#include <hip/hip_bf16.h>
#include <stdint.h>
#include <type_traits>
#include <utility>
#define LAS __attribute__((address_space(3)))
#define GAS __attribute__((address_space(1)))
#define DI __device__ __forceinline__
typedef unsigned short u16;
typedef unsigned long long u64;
typedef short s16x4 __attribute__((ext_vector_type(4)));
typedef short s16x8 __attribute__((ext_vector_type(8)));
typedef __bf16 bf16x2 __attribute__((ext_vector_type(2)));
typedef float f32x2 __attribute__((ext_vector_type(2)));
typedef float f32x4 __attribute__((ext_vector_type(4)));
typedef unsigned u32x4 __attribute__((ext_vector_type(4)));
typedef unsigned u32x2 __attribute__((ext_vector_type(2)));
constexpr int S_=2048, D_=640, F_=1728;
#define ROT 0
#define NTX 0
#define NTSX 0
#define NTSW 0
#define NTCS 0
#define TKM 0
#define PORD 0
#define GAP() __builtin_amdgcn_sched_barrier(0)
DI unsigned rfl(unsigned x){ return __builtin_amdgcn_readfirstlane(x); }
DI u32x4 mk_rsrc(const void* p, unsigned bytes){
  unsigned long long a=(unsigned long long)p;
  u32x4 r; r[0]=rfl((unsigned)a); r[1]=rfl((unsigned)(a>>32))&0xffffu; r[2]=bytes; r[3]=0x00020000u; return r;
}
DI void setm0(unsigned x){ asm volatile("s_mov_b32 m0, %0\n\ts_nop 0"::"s"(x)); }
template<int I> DI void dma (unsigned vo,const u32x4& r,unsigned so){ asm volatile("buffer_load_dwordx4 %0, %1, %2 offen offset:%3 lds"::"v"(vo),"s"(r),"s"(so),"n"(I)); }
template<int I> DI void dmas(unsigned vo,const u32x4& r,unsigned so){ asm volatile("buffer_load_dwordx4 %0, %1, %2 offen offset:%3 sc1 lds"::"v"(vo),"s"(r),"s"(so),"n"(I)); }
template<int I> DI void ld4  (u32x4& d,unsigned vo,const u32x4& r,unsigned so){ asm volatile("buffer_load_dwordx4 %0, %1, %2, %3 offen offset:%4":"=v"(d):"v"(vo),"s"(r),"s"(so),"n"(I)); }
template<int I> DI void dman(unsigned vo,const u32x4& r,unsigned so){ asm volatile("buffer_load_dwordx4 %0, %1, %2 offen offset:%3 nt lds"::"v"(vo),"s"(r),"s"(so),"n"(I)); }
template<int I> DI void ld4n (u32x4& d,unsigned vo,const u32x4& r,unsigned so){ asm volatile("buffer_load_dwordx4 %0, %1, %2, %3 offen offset:%4 nt":"=v"(d):"v"(vo),"s"(r),"s"(so),"n"(I)); }
template<int N,int I> DI void ldwa(unsigned vo,const u32x4& r,unsigned so){ asm volatile("buffer_load_dwordx4 a[%c3:%c4], %0, %1, %2 offen offset:%5 sc1"::"v"(vo),"s"(r),"s"(so),"n"(4*N),"n"(4*N+3),"n"(I)); }
template<int N> DI void mfmaw (f32x4& acc,const u32x4& b){ asm volatile("v_mfma_f32_16x16x32_bf16 %0, a[%c2:%c3], %1, %0":"+v"(acc):"v"(b),"n"(4*N),"n"(4*N+3)); }
template<int N> DI void mfmawz(f32x4& acc,const u32x4& b){ asm volatile("v_mfma_f32_16x16x32_bf16 %0, a[%c2:%c3], %1, 0":"=&v"(acc):"v"(b),"n"(4*N),"n"(4*N+3)); }
template<int NT,int I> DI void dmap(unsigned vo,const u32x4& r,unsigned so){ if constexpr(NT) dman<I>(vo,r,so); else dma<I>(vo,r,so); }
template<int NT,int I> DI void ld4p(u32x4& d,unsigned vo,const u32x4& r,unsigned so){ if constexpr(NT) ld4n<I>(d,vo,r,so); else ld4<I>(d,vo,r,so); }
template<int I> DI void ld1n (unsigned& d,unsigned vo,const u32x4& r,unsigned so){ asm volatile("buffer_load_dword %0, %1, %2, %3 offen offset:%4 nt":"=v"(d):"v"(vo),"s"(r),"s"(so),"n"(I)); }
template<int I> DI void ld4s (u32x4& d,unsigned vo,const u32x4& r,unsigned so){ asm volatile("buffer_load_dwordx4 %0, %1, %2, %3 offen offset:%4 sc1":"=v"(d):"v"(vo),"s"(r),"s"(so),"n"(I)); }
template<int I> DI void ld4sa(u32x4& d,unsigned vo,const u32x4& r,unsigned so){ asm volatile("buffer_load_dwordx4 %0, %1, %2, %3 offen offset:%4 sc1":"=a"(d):"v"(vo),"s"(r),"s"(so),"n"(I)); }
template<int I> DI void ld2s (u32x2& d,unsigned vo,const u32x4& r,unsigned so){ asm volatile("buffer_load_dwordx2 %0, %1, %2, %3 offen offset:%4 sc1":"=v"(d):"v"(vo),"s"(r),"s"(so),"n"(I)); }
template<int I> DI void ld2  (u32x2& d,unsigned vo,const u32x4& r,unsigned so){ asm volatile("buffer_load_dwordx2 %0, %1, %2, %3 offen offset:%4":"=v"(d):"v"(vo),"s"(r),"s"(so),"n"(I)); }
template<int I> DI void ld1s (unsigned& d,unsigned vo,const u32x4& r,unsigned so){ asm volatile("buffer_load_dword %0, %1, %2, %3 offen offset:%4 sc1":"=v"(d):"v"(vo),"s"(r),"s"(so),"n"(I)); }
template<int I> DI void ld1  (unsigned& d,unsigned vo,const u32x4& r,unsigned so){ asm volatile("buffer_load_dword %0, %1, %2, %3 offen offset:%4":"=v"(d):"v"(vo),"s"(r),"s"(so),"n"(I)); }
template<int I> DI void st4s (const u32x4& d,unsigned vo,const u32x4& r,unsigned so){ asm volatile("buffer_store_dwordx4 %0, %1, %2, %3 offen offset:%4 sc1\n\ts_nop 1"::"v"(d),"v"(vo),"s"(r),"s"(so),"n"(I)); }
template<int I> DI void st2s (const u32x2& d,unsigned vo,const u32x4& r,unsigned so){ asm volatile("buffer_store_dwordx2 %0, %1, %2, %3 offen offset:%4 sc1\n\ts_nop 1"::"v"(d),"v"(vo),"s"(r),"s"(so),"n"(I)); }
#define SETM0(x) setm0(x)
#define DMA(voff,rsrc,soff,imm) dma<imm>(voff,rsrc,soff)
#define DMAS(voff,rsrc,soff,imm) dmas<imm>(voff,rsrc,soff)
#define LD4(dst,voff,rsrc,soff,imm) ld4<imm>(dst,voff,rsrc,soff)
#define LD4S(dst,voff,rsrc,soff,imm) ld4s<imm>(dst,voff,rsrc,soff)
#define LD4SA(dst,voff,rsrc,soff,imm) ld4sa<imm>(dst,voff,rsrc,soff)
#define LD2S(dst,voff,rsrc,soff,imm) ld2s<imm>(dst,voff,rsrc,soff)
#define LD2(dst,voff,rsrc,soff,imm) ld2<imm>(dst,voff,rsrc,soff)
#define LD1S(dst,voff,rsrc,soff,imm) ld1s<imm>(dst,voff,rsrc,soff)
#define LD1(dst,voff,rsrc,soff,imm) ld1<imm>(dst,voff,rsrc,soff)
#define ST4S(src,voff,rsrc,soff,imm) st4s<imm>(src,voff,rsrc,soff)
#define ST2S(src,voff,rsrc,soff,imm) st2s<imm>(src,voff,rsrc,soff)
template<int N> DI void waitv(){ asm volatile("s_waitcnt vmcnt(%0)"::"n"(N):"memory"); }
template<class T> DI void pin(T& a){ asm volatile("":"+v"(a)::"memory"); }
template<class T,class... R> DI void pin(T& a,R&... r){ pin(a); pin(r...); }
template<int N,class... R> DI void waitv(R&... r){ asm volatile("s_waitcnt vmcnt(%0)"::"n"(N):"memory"); pin(r...); }
#define WAITV(n) waitv<n>()
#define WAITL() asm volatile("s_waitcnt lgkmcnt(0)":::"memory")
#define BARRIER() asm volatile("s_waitcnt lgkmcnt(0)\n\ts_barrier":::"memory")
DI void MFMA16  (f32x4& acc,const u32x4& a,const u32x4& b){ asm volatile("v_mfma_f32_16x16x32_bf16 %0, %1, %2, %0":"+v"(acc):"v"(a),"v"(b)); }
DI void MFMA16Z (f32x4& acc,const u32x4& a,const u32x4& b){ asm volatile("v_mfma_f32_16x16x32_bf16 %0, %1, %2, 0":"=&v"(acc):"v"(a),"v"(b)); }
DI void MFMA16A (f32x4& acc,const u32x4& a,const u32x4& b){ asm volatile("v_mfma_f32_16x16x32_bf16 %0, %1, %2, %0":"+v"(acc):"a"(a),"v"(b)); }
DI void MFMA16AZ(f32x4& acc,const u32x4& a,const u32x4& b){ asm volatile("v_mfma_f32_16x16x32_bf16 %0, %1, %2, 0":"=&v"(acc):"a"(a),"v"(b)); }
DI void NOP8(){ asm volatile("s_nop 7":::"memory"); __builtin_amdgcn_sched_barrier(0); }
DI float bflo(unsigned x){ return __uint_as_float(x<<16); }
DI float bfhi(unsigned x){ return __uint_as_float(x&0xffff0000u); }
DI unsigned cvtpk(float lo,float hi){ f32x2 v={lo,hi}; bf16x2 b=__builtin_convertvector(v,bf16x2); return __builtin_bit_cast(unsigned,b); }
DI float rbf(float x){ return bflo(cvtpk(x,x)); }
DI u32x4 cat(s16x4 a, s16x4 b){ s16x8 r=__builtin_shufflevector(a,b,0,1,2,3,4,5,6,7); return __builtin_bit_cast(u32x4,r); }
DI u32x4 cat2(u32x2 a,u32x2 b){ return __builtin_shufflevector(a,b,0,1,2,3); }
DI s16x4 trread(unsigned a){ return __builtin_amdgcn_ds_read_tr16_b64_v4i16((LAS s16x4*)(size_t)a); }
DI u32x4 lr128(unsigned a){ return *(const LAS u32x4*)(size_t)a; }
DI f32x4 lr128f(unsigned a){ return *(const LAS f32x4*)(size_t)a; }
DI u32x2 lr64v(unsigned a){ return *(const volatile LAS u32x2*)(size_t)a; }
DI void lw128(unsigned a,u32x4 v){ *(LAS u32x4*)(size_t)a=v; }
DI void lw128f(unsigned a,f32x4 v){ *(LAS f32x4*)(size_t)a=v; }
DI void lw64(unsigned a,u32x2 v){ *(LAS u32x2*)(size_t)a=v; }
DI unsigned opq(unsigned x){ asm volatile("":"+v"(x)); return x; }
template<int CTRL> DI float dpp_add(float v){ return v+__builtin_bit_cast(float,__builtin_amdgcn_update_dpp(0,__builtin_bit_cast(int,v),CTRL,0xf,0xf,true)); }
DI float sum32(float v){
  v=dpp_add<0x128>(v); v=dpp_add<0x124>(v); v=dpp_add<0x122>(v); v=dpp_add<0x121>(v);
  auto r=__builtin_amdgcn_permlane16_swap(__float_as_uint(v),__float_as_uint(v),false,false);
  return __uint_as_float(r[0])+__uint_as_float(r[1]); }

constexpr unsigned OOB=0xfff00000u;
template<int... I,class F> DI void sfor_impl(std::integer_sequence<int,I...>,F&& f){ (f(std::integral_constant<int,I>{}),...); }
template<int N,class F> DI void sfor(F&& f){ sfor_impl(std::make_integer_sequence<int,N>{},f); }
#define SFOR(N,v) sfor<N>([&](auto v##_) __attribute__((always_inline)) { constexpr int v=v##_;
#define SEND });
#define IC(v) std::integral_constant<int,(v)>{}
constexpr unsigned WS_E=0, WS_FP=256, WS_FQ=WS_FP+2048, WS_FH=WS_FQ+256, WS_FY=WS_FH+8192, WS_FLAGS_END=WS_FY+10240;
constexpr unsigned WS_ST=24576  , WS_CS=WS_ST+16384  , WS_UT=WS_CS+57344, WS_WDF=WS_UT+D_*S_*2, WS_HF=WS_WDF+F_*D_*2, WS_YT=WS_HF+S_*F_*2, WS_END=WS_YT+D_*S_*2;
constexpr unsigned WS_DBG=32u<<20;
static_assert(WS_FLAGS_END<=WS_ST,"");
constexpr int STG=49152;
constexpr int L_KV=2*STG, L_CSD=2*STG+16384;
constexpr int L_TAB=3*STG;
constexpr int CV_HC=4160, CV_H=4*CV_HC, CV_U=4608, CV_CH=CV_H+CV_U;
constexpr int L_CV=STG;
constexpr int L_RED=2*STG;
constexpr int L_Y=L_TAB;
constexpr int LDS_BYTES=163840;

#define STAMP(i)

#define STAMP2(i)
#define STAMPM(i) STAMP(i)
#define STAMP5(i)
#define STAMP4(i)
#define STAMP3(i)
#define STAMPL(i) STAMP(i)
DI void spin4(const u32x4& rws, unsigned off, int nlanes, u64 tag){
  const int lane=threadIdx.x&63; const unsigned vo=(off+lane*32)|(lane<nlanes?0:OOB); const unsigned tl=(unsigned)tag, th=(unsigned)(tag>>32);
  const u64 t0=__builtin_amdgcn_s_memrealtime();
  for(;;){
    u32x4 a,c; ld4s<0>(a,vo,rws,0); ld4s<16>(c,vo,rws,0); waitv<0>(a,c);
    const bool ok= a[0]==tl&&a[1]==th&&a[2]==tl&&a[3]==th&&c[0]==tl&&c[1]==th&&c[2]==tl&&c[3]==th;
    if(__all(ok || lane>=nlanes)) break;
    if(__builtin_amdgcn_s_memrealtime()-t0>2000000ull) break;
    __builtin_amdgcn_s_sleep(1);
  }
}
DI void put_flag(char* ws, unsigned off, u64 tag){ __hip_atomic_store((GAS u64*)(ws+off),tag,__ATOMIC_RELAXED,__HIP_MEMORY_SCOPE_AGENT); }
DI bool tag4(u32x4 v,u64 tag){ const unsigned tl=(unsigned)tag, th=(unsigned)(tag>>32); return v[0]==tl&&v[1]==th&&v[2]==tl&&v[3]==th; }

__global__ __launch_bounds__(256,1) void fused_kernel(const u16* __restrict__ x,const u16* __restrict__ kk,const u16* __restrict__ vv,const u16* __restrict__ sk,
    const u16* __restrict__ hk,const u16* __restrict__ Dv,const u16* __restrict__ wg,const u16* __restrict__ wu,const u16* __restrict__ wd,u16* __restrict__ out,char* ws){
  extern __shared__ __attribute__((aligned(16))) char smem[];
  STAMP(0);
  if(threadIdx.x==0) __hip_atomic_store((GAS u64*)(ws+(40u<<20))+blockIdx.x,(u64)__builtin_amdgcn_s_memrealtime(),__ATOMIC_RELAXED,__HIP_MEMORY_SCOPE_AGENT);
  asm volatile("":::"a0","a255");
  asm volatile(""::"s"(x),"s"(kk),"s"(vv),"s"(sk),"s"(hk),"s"(Dv),"s"(wg),"s"(wu),"s"(wd),"s"(out),"s"(ws));
  const int tid=threadIdx.x, lane=tid&63; const unsigned w=rfl(tid>>6); const int b=blockIdx.x;
  const unsigned lds0=(unsigned)(size_t)(LAS char*)smem;
  const u32x4 rws=mk_rsrc(ws,WS_END), rx=mk_rsrc(x,S_*D_*2);
  u32x2 Eraw; { const unsigned z=0; LD2S(Eraw,z,rws,0,0); }
  const int lrow=8*b+2*w+(lane>>5), l32=lane&31;
  u32x4 xc3[3];
  { const unsigned vo=lrow*1280+l32*16; ld4p<NTX,0>(xc3[0],vo,rx,0); ld4p<NTX,512>(xc3[1],vo,rx,0); const unsigned v2=(vo+1024)|(l32<16?0:OOB); ld4p<NTX,0>(xc3[2],v2,rx,0); }
  STAMP3(6);
  constexpr int NPF1=0;
  constexpr int NPF=NPF1;
  auto pinpf=[&]() __attribute__((always_inline)) {
  };
  const int xcd=b&7, jj=b>>3; const bool isg=jj<28;
  const int g1id=xcd*28+jj, spid=xcd*4+jj-28;
  const int mi=(xcd>>1)*4+(jj&3), ni=(xcd&1)*7+(jj>>2);
  const int gj=lane&15,gq=lane>>4;
  const bool iskv=isg && g1id>=64; const int kvt=g1id-64;
  const int tb=kvt/5, cb=kvt-5*tb, t0=tb*64, pc_=tid&15, tg=tid>>4, d0=cb*128+pc_*8;
  STAMP3(7);
  const u32x4 rg=mk_rsrc(wg,D_*F_*2), ru=mk_rsrc(wu,D_*F_*2);
  unsigned vx[4], vw[4];
  #pragma unroll
  for(int i=0;i<4;i++){ const int row=32*w+8*i+(lane>>3); const int c=(lane&7)^((row>>1)&7); vx[i]=(128*mi+row)*1280+c*16-1024*i;
    const int k=16*w+4*i+(lane>>4), cp=lane&15, gs=(cp>>1)^((k&3)|(((k>>3)&1)<<2)); vw[i]=k*3456+ni*256+gs*32+(cp&1)*16-1024*i; }
  const unsigned m0x=rfl(lds0+w*4096), m0g=m0x+16384, m0u=m0x+32768;
  auto piece=[&](int P,int st,int bf) __attribute__((always_inline)) {
    if(P==0) SETM0(m0x+bf*STG); if(P==4) SETM0(m0g+bf*STG); if(P==8) SETM0(m0u+bf*STG);
    const unsigned sx=st*128, sw=st*221184;
    if(P==0) dmap<NTSX,0>(vx[0],rx,sx); if(P==1) dmap<NTSX,1024>(vx[1],rx,sx); if(P==2) dmap<NTSX,2048>(vx[2],rx,sx); if(P==3) dmap<NTSX,3072>(vx[3],rx,sx);
    if(P==4) dmap<NTSW,0>(vw[0],rg,sw); if(P==5) dmap<NTSW,1024>(vw[1],rg,sw); if(P==6) dmap<NTSW,2048>(vw[2],rg,sw); if(P==7) dmap<NTSW,3072>(vw[3],rg,sw);
    if(P==8) dmap<NTSW,0>(vw[0],ru,sw); if(P==9) dmap<NTSW,1024>(vw[1],ru,sw); if(P==10) dmap<NTSW,2048>(vw[2],ru,sw); if(P==11) dmap<NTSW,3072>(vw[3],ru,sw);
  };
  u64 tag;
  auto stats=[&]() __attribute__((always_inline)) {
  tag=((u64)rfl(Eraw[0])|((u64)rfl(Eraw[1])<<32))*6364136223846793005ull+1442695040888963407ull;
  {
    f32x2 f[12];
    #pragma unroll
    for(int i=0;i<3;i++) for(int e=0;e<4;e++) f[4*i+e]=f32x2{bflo(xc3[i][e]),bfhi(xc3[i][e])};
    f32x2 s0=(f[0]+f[1])+(f[2]+f[3]), s1=(f[4]+f[5])+(f[6]+f[7]), s2=(f[8]+f[9])+(f[10]+f[11]); s0=(s0+s1)+s2;
    const float mean=sum32(s0[0]+s0[1])*(1.f/640.f);
    f32x2 q0={0.f,0.f},q1={0.f,0.f},q2={0.f,0.f};
    #pragma unroll
    for(int i=0;i<4;i++){ f32x2 d=f[i]-mean, e=f[4+i]-mean, g=f[8+i]-mean; q0=d*d+q0; q1=e*e+q1; q2=g*g+q2; }
    q0+=q1;
    const float var=rbf(sum32(q0[0]+q0[1]+(l32<16? q2[0]+q2[1]:0.f))*(1.f/640.f)); const float m=rbf(mean);
    const float den=rbf(__builtin_amdgcn_sqrtf(rbf(var+1e-5f)));
    u32x2 sv={__float_as_uint(m),__float_as_uint(__builtin_amdgcn_rcpf(den))}; const unsigned vo=(WS_ST+lrow*8)|(l32==0?0:OOB);
    ST2S(sv,vo,rws,0,0);
  }
  };
  const int mt=(xcd>>1)*8+(jj&7), nt=(xcd&1)*4+(jj>>3);
  const u32x4 rhk=mk_rsrc(hk,D_*S_*2);
  const int cvp=w>>1, cvh=w&1;
  const unsigned ldsC=rfl(lds0+L_CV+cvp*CV_CH);
  const int chl=128+2*b+cvp;
  const unsigned vw0=lane*16, vw1=vw0+4096;
  const int kst=14*w-(w==3?1:0);
  const unsigned wdbase=rfl(WS_WDF+(nt*54+kst)*5120), hbase=rfl((4*mt*54+kst)*1024);
  const unsigned wd13=rfl(w<2? wdbase+13*5120 : 0x10000000u), h13=rfl(w<2? hbase+13*1024 : 0x10000000u);
  auto ldwd=[&](auto NC) __attribute__((always_inline)) { constexpr int n=decltype(NC)::value, i=n/5, dt=n%5; static_assert(i<13,"");
    const unsigned so=wdbase+i*5120; if constexpr(dt<4) ldwa<n,dt*1024>(vw0,rws,so); else ldwa<n,0>(vw1,rws,so); };
  auto conv_issue=[&](unsigned base,int ch,int part) __attribute__((always_inline)) {
    unsigned so=rfl(ch*4096); asm volatile("s_nop 4":"+s"(so));
    if(part<2){ const int c=2*cvh+part; const unsigned vo=lane*16+2*c; SETM0(base+c*CV_HC+32); dman<0>(vo,rhk,so); dman<1024>(vo,rhk,so); dman<2048>(vo,rhk,so); dman<3072>(vo,rhk,so); }
    else { const unsigned vo=WS_UT+lane*16+cvh*2048; SETM0(base+CV_H+cvh*2048); DMAS(vo,rws,so,0); DMAS(vo,rws,so,1024); }
  };
  const unsigned ldsR0=rfl(lds0+cvp*CV_CH), ldsR1=rfl(lds0+2*STG+16384+cvp*CV_CH);
  constexpr int NWE=44;
  if(isg){
    auto middle=[&](auto NLC, unsigned& tk) __attribute__((always_inline)) { constexpr int NL=decltype(NLC)::value;
      { const unsigned vo=((128+2*b)*4096+lane*128)|((w==0&&TKM)?0:OOB); if constexpr(TKM==2) ld1n<0>(tk,vo,rhk,0); else LD1(tk,vo,rhk,0,0); }
      waitv<NL+1+NPF>(Eraw,xc3[0],xc3[1],xc3[2]);
      STAMP3(8);
      stats();
      STAMPM(1);
      #pragma unroll
      for(int P=0;P<12;P++) piece(P,0,0); };
    const unsigned P=lds0+L_KV;
    if(iskv){
      u32x4 pr[15];
      const u32x4 rk=mk_rsrc(kk,S_*D_*2), rv=mk_rsrc(vv,S_*D_*2), rsk=mk_rsrc(sk,D_*8);
      const unsigned vb=((t0+4*tg)*D_+d0)*2, va=(vb-3840)|((t0+tg==0)?OOB:0), vs=d0*8;
      ld4n<0>(pr[0],va,rk,0); ld4n<1280>(pr[1],va,rk,0); ld4n<2560>(pr[2],va,rk,0);
      ld4n<0>(pr[3],vb,rk,0); ld4n<1280>(pr[4],vb,rk,0); ld4n<2560>(pr[5],vb,rk,0); ld4n<3840>(pr[6],vb,rk,0);
      ld4n<0>(pr[7],vb,rv,0); ld4n<1280>(pr[8],vb,rv,0); ld4n<2560>(pr[9],vb,rv,0); ld4n<3840>(pr[10],vb,rv,0);
      ld4<0>(pr[11],vs,rsk,0); ld4<16>(pr[12],vs,rsk,0); ld4<32>(pr[13],vs,rsk,0); ld4<48>(pr[14],vs,rsk,0);
      unsigned vu=opq(WS_UT+(cb*128+(tid>>3))*4096+(S_-64-t0+(tid&7)*8)*2), lw_=opq(P+8*pc_*128+((15-tg)^pc_)*8);
      unsigned tk; middle(IC(15),tk);
      waitv<13>(pr[0],pr[1],pr[2],pr[3],pr[4],pr[5],pr[6],pr[7],pr[8],pr[9],pr[10],pr[11],pr[12],pr[13],pr[14],tk);
      pinpf();
      STAMP3(9);
      #pragma unroll
      for(int e2=0;e2<4;e2++){
        piece(3*e2,1,1); piece(3*e2+1,1,1); piece(3*e2+2,1,1);
        const f32x2 w0={bflo(pr[11+e2][0]),bflo(pr[11+e2][2])}, w1={bfhi(pr[11+e2][0]),bfhi(pr[11+e2][2])}, w2={bflo(pr[11+e2][1]),bflo(pr[11+e2][3])}, w3={bfhi(pr[11+e2][1]),bfhi(pr[11+e2][3])};
        f32x2 kf[7];
        #pragma unroll
        for(int i=0;i<7;i++) kf[i]=f32x2{bflo(pr[i][e2]),bfhi(pr[i][e2])};
        f32x2 o[4];
        #pragma unroll
        for(int i=0;i<4;i++){ f32x2 ks=kf[i]*w3; ks=kf[i+1]*w2+ks; ks=kf[i+2]*w1+ks; ks=kf[i+3]*w0+ks; o[i]=ks*f32x2{bflo(pr[7+i][e2]),bfhi(pr[7+i][e2])}; }
        #pragma unroll
        for(int z=0;z<2;z++){ u32x2 pk={cvtpk(o[3][z],o[2][z]),cvtpk(o[1][z],o[0][z])}; lw64(lw_+(2*e2+z)*128,pk); }
      }
      STAMP3(10);
      BARRIER();
      STAMP3(11);
      u32x4 v[4];
      #pragma unroll
      for(int i=0;i<4;i++) v[i]=lr128(P+((tid>>3)+32*i)*128+(((tid&7)^((w>>1)+2*i))*16));
      #pragma unroll
      for(int i=0;i<4;i++){ const u32x4 o= (w&1)? u32x4{v[i][2],v[i][3],v[i][0],v[i][1]} : v[i]; st4s<0>(o,vu,rws,i*131072); }
    } else {
      u32x4 pr[12];
      const u32x4 rwd=mk_rsrc(wd,F_*D_*2);
      const unsigned vo=(lane>>3)*1280+(lane&7)*16; unsigned so[3], sd[3]; int c4[3];
      #pragma unroll
      for(int z=0;z<3;z++){ const int un=4*g1id+w+256*z, s=un/10, c64=un-10*s; so[z]=rfl(un<540? (32*s*D_+c64*64)*2 : 0x10000000); sd[z]=rfl(un<540? WS_WDF+s*5120 : 0x10000000); c4[z]=4*c64; }
      SFOR(12,i) ld4n<0>(pr[i],vo,rwd,so[i>>2]+(i&3)*8*1280); SEND
      unsigned tk; middle(IC(12),tk);
      waitv<13>(pr[0],pr[1],pr[2],pr[3],pr[4],pr[5],pr[6],pr[7],pr[8],pr[9],pr[10],pr[11],tk);
      pinpf();
      const unsigned L=P+w*12288;
      #pragma unroll
      for(int z=0;z<3;z++){
        #pragma unroll
        for(int i=0;i<4;i++){ piece(4*z+i,1,1); const int r=8*i+(lane>>3); lw128(L+z*4096+r*128+(((lane&7)^((r>>2)&7))*16),pr[4*z+i]); } }
      BARRIER();
      const int qq=(lane&15)>>2, p=lane&3, r=4*gq+qq;
      SFOR(12,i) constexpr int z=i>>2, f4=i&3; const int ch=2*f4+(p>>1);
        const u32x4 v=cat(trread(L+z*4096+r*128+((ch^((r>>2)&7))*16)+(p&1)*8),trread(L+z*4096+(r+16)*128+((ch^(((r+16)>>2)&7))*16)+(p&1)*8));
        const int D16=c4[z]+f4, n5=D16/5, dt=D16-5*n5; unsigned so2=rfl(sd[z]+n5*276480+dt*1024); asm volatile("s_nop 4":"+s"(so2)); st4s<0>(v,vw0,rws,so2); SEND
    }
    STAMPM(2);
    const int wm=w>>1, wn=w&1;
    unsigned xa[2], wa[4];
    { const int qq=(lane&15)>>2, p=lane&3;
      #pragma unroll
      for(int j=0;j<2;j++) xa[j]=opq(lds0+(64*wm+gj)*128+(((4*j+gq)^((lane>>1)&7))*16));
      #pragma unroll
      for(int z=0;z<4;z++) wa[z]=opq(lds0+16384+(8*gq+qq)*256+(((4*wn+z)^(qq|((gq&1)<<2)))*32)+p*8); }
    f32x4 ag[4][4], au[4][4];
    u32x4 fw[4], fx[2][4];
    u32x4 fp0,fp1; u32x2 fq, st[4]; unsigned csp[2];
    auto rdw=[&](auto GC,auto JC,auto ZC,auto HC) __attribute__((always_inline)) {
      constexpr int G=decltype(GC)::value, J=decltype(JC)::value, Z=decltype(ZC)::value, Hh=decltype(HC)::value;
      const u32x2 t=__builtin_bit_cast(u32x2,trread(wa[Z&3]+J*8192+(Z>>2)*16384+Hh*1024));
      if constexpr(Hh==0){ u32x4 v={t[0],t[1],t[0],t[1]}; fw[G&3]=v; } else { fw[G&3][2]=t[0]; fw[G&3][3]=t[1]; } };
        WAITV(16); BARRIER();
    #pragma unroll
    for(int m=0;m<4;m++) fx[0][m]=lr128(xa[0]+m*2048);
    SFOR(3,z) rdw(IC(z),IC(0),IC(z),IC(0)); rdw(IC(z),IC(0),IC(z),IC(1)); SEND
    STAMPM(3); STAMP5(1);
    SFOR(10,s)
      SFOR(2,j)
        constexpr int c=j&1,n=c^1;
        SFOR(8,z)
          constexpr int G=(2*s+j)*8+z;
          SFOR(4,m)
            f32x4& A= z<4? ag[z&3][m] : au[z&3][m];
            if constexpr(s==0&&j==0) MFMA16Z(A,fw[G&3],fx[c][m]); else MFMA16(A,fw[G&3],fx[c][m]);
            if constexpr(j==0){
              if constexpr(m<2){ if constexpr(z<5) rdw(IC(G+3),IC(0),IC(z+3),IC(m)); else rdw(IC(G+3),IC(1),IC(z-5),IC(m)); }
              if constexpr(m==2 && z>=4) fx[n][z-4]=lr128(xa[1]+(z-4)*2048);
            } else {
              if constexpr(z<5 && m<2) rdw(IC(G+3),IC(1),IC(z+3),IC(m));
              if constexpr(s<9 && z==6){ if constexpr(m<2){ rdw(IC(G+2),IC(0),IC(0),IC(m)); fx[n][m]=lr128(xa[0]+m*2048); } else rdw(IC(G+3),IC(0),IC(1),IC(m-2)); }
              if constexpr(s<9 && z==7 && m<2){ rdw(IC(G+3),IC(0),IC(2),IC(m)); fx[n][2+m]=lr128(xa[0]+(2+m)*2048); }
            }
            if constexpr(s==0&&j==0){ if constexpr(z<6 && (m&1)) piece(2*z+(m>>1),2,2); }
            else if constexpr(m==3){
              if constexpr(j==1 && z>=6 && s+3<10) piece(z-6,s+3,(s+3)%3);
              if constexpr(j==0 && s+2<10) piece(2+z,s+2,(s+2)%3);
              if constexpr(j==1 && z<2 && s>=1 && s+2<10) piece(10+z,s+2,(s+2)%3);
              if constexpr(s==7 && j==1 && z==2){ const unsigned vo=WS_FP+lane*32; LD4S(fp0,vo,rws,0,0); LD4S(fp1,vo,rws,0,16);
                const unsigned vq=(WS_FQ+lane*8)|(lane<32?0:OOB); LD2S(fq,vq,rws,0,0); }
              if constexpr(s==8 && j==0 && (z==1||z==5)) conv_issue(ldsC,chl,z>>2);
            }
            GAP();
          SEND
          if constexpr(j==1 && z==5 && s<9){
            if constexpr(s==0) WAITV(16); else if constexpr(s<=6) WAITV(12); else if constexpr(s==7) WAITV(15); else WAITV(8);
            if constexpr(s==8){
              pin(fp0,fp1,fq);
              if(!__all(tag4(fp0,tag)&&tag4(fp1,tag)&&((fq[0]==(unsigned)tag&&fq[1]==(unsigned)(tag>>32))||lane>=32))){
                spin4(rws,WS_FP,64,tag); spin4(rws,WS_FQ,8,tag); }
              if(b==0 && tid==0) put_flag(ws,WS_E,tag);
              #pragma unroll
              for(int m=0;m<4;m++){ const unsigned vo=WS_ST+(128*mi+64*wm+16*m+gj)*8; LD2S(st[m],vo,rws,0,0); }
              const int col= tid<128? ni*128+tid : 1728+ni*128+tid-128; const unsigned vo=(WS_CS+col*4)|(((tid&127)+ni*128<1728)?0:OOB);
              LD1S(csp[0],vo,rws,0,0); { const unsigned so=13824; LD1S(csp[1],vo,rws,so,0); }
              conv_issue(ldsC,chl,2);
            }
            BARRIER();
            STAMP5(s+2);
            if constexpr(s==1) if(tid==0) put_flag(ws,WS_FP+b*8,tag);
            constexpr int dl= (s+1)%3==0? -2*STG : STG;
            xa[0]=opq(xa[0]+dl); xa[1]=opq(xa[1]+dl); wa[0]=opq(wa[0]+dl); wa[1]=opq(wa[1]+dl); wa[2]=opq(wa[2]+dl); wa[3]=opq(wa[3]+dl);
            GAP();
          }
        SEND
      SEND
    SEND
    STAMPM(4); STAMP5(11);
    waitv<2>(csp[0],csp[1],st[0],st[1],st[2],st[3]);
    *(LAS float*)(size_t)(lds0+L_TAB+tid*4)=__uint_as_float(csp[0])+__uint_as_float(csp[1]);
    BARRIER();
    NOP8();
    { const float L2E=1.4426950408889634f;
      f32x4 cg[4], cu[4];
      #pragma unroll
      for(int z=0;z<4;z++){ cg[z]=lr128f(lds0+L_TAB+(64*wn+16*z+4*gq)*4); cu[z]=lr128f(lds0+L_TAB+512+(64*wn+16*z+4*gq)*4); }
      SFOR(4,i) ldwd(IC(i)); SEND
      SFOR(4,m)
        const float mean=__uint_as_float(st[m][0]), r=__uint_as_float(st[m][1]);
        const float R=-L2E*r, C=-r*(1.f/L2E), nm=-mean;
        SFOR(2,G)
          u32x4 o;
          SFOR(4,te)
            constexpr int T=te>>1, e=te&1, z=2*G+T;
            f32x2 g={ag[z][m][2*e],ag[z][m][2*e+1]}, u={au[z][m][2*e],au[z][m][2*e+1]};
            g=f32x2{cg[z][2*e],cg[z][2*e+1]}*nm+g; u=f32x2{cu[z][2*e],cu[z][2*e+1]}*nm+u;
            f32x2 s=g*R; f32x2 ex={__builtin_amdgcn_exp2f(s[0]),__builtin_amdgcn_exp2f(s[1])};
            f32x2 d=ex+1.f; f32x2 rc={__builtin_amdgcn_rcpf(d[0]),__builtin_amdgcn_rcpf(d[1])};
            f32x2 h=((u*C)*s)*rc; o[2*T+e]=cvtpk(h[0],h[1]);
            ldwd(IC(4+(m*2+G)*5+te));
            GAP();
          SEND
          const int s32=ni*4+2*wn+G, m16=mi*8+wm*4+m;
          const unsigned vo=(WS_HF+((m16*54+s32)*64+lane)*16)|(s32<54?0:OOB); ST4S(o,vo,rws,0,0);
          ldwd(IC(4+(m*2+G)*5+4));
        SEND
      SEND }
    STAMPM(5);
    WAITV(52);
  } else {
    unsigned tk0;
    {
      const int gw=4*spid+w, cbk=gw>>1, kh=gw&1; const bool act=gw<108;
      const u32x4 rcs=mk_rsrc(cbk<27? wg:wu, D_*F_*2); const int cb64=cbk<27? cbk: cbk-27;
      unsigned vc[2];
      #pragma unroll
      for(int i=0;i<2;i++){ const int r=8*i+(lane>>3), cp=lane&7; const int gs=(cp>>1)^(((r>>1)&1)|((i&1)<<1)); vc[i]=((320*kh+r)*3456+cb64*128+gs*32+(cp&1)*16)|(act?0:OOB); }
      const unsigned LB=rfl(lds0+w*16384);
      auto csissue=[&](int c) __attribute__((always_inline)) {
        #pragma unroll
        for(int i=0;i<8;i++){ SETM0(LB+(c&1)*8192+i*1024); const unsigned so=(64*c+16*(i>>1))*3456; dmap<NTCS,0>(vc[i&1],rcs,so); } };
      { const unsigned vo=((w<2? 4*spid+2*w : 128+2*b)*4096+lane*128)|((w<3&&TKM)?0:OOB); if constexpr(TKM==2) ld1n<0>(tk0,vo,rhk,0); else LD1(tk0,vo,rhk,0,0); }
      csissue(0); csissue(1);
      waitv<17+NPF>(Eraw,xc3[0],xc3[1],xc3[2]);
      stats();
      STAMPM(1);
      WAITV(0); pinpf();
      BARRIER();
      if(tid==0) put_flag(ws,WS_FP+b*8,tag);
      const int qq=(lane&15)>>2, p=lane&3; f32x4 cs[4]; u32x4 on={0x3f803f80u,0x3f803f80u,0x3f803f80u,0x3f803f80u}; asm volatile("s_nop 1":"+v"(on));
      SFOR(5,c)
        if constexpr(c>=1 && c<4) WAITV(8); else if constexpr(c==4) WAITV(0);
        u32x4 fa[2][4];
        SFOR(2,k) SFOR(4,z) const int r0=32*k+8*gq+qq; const int g0=((r0>>1)&1)|(((r0>>3)&1)<<1);
          fa[k][z]=cat(trread(LB+(c&1)*8192+r0*128+((z^g0)*32)+p*8),trread(LB+(c&1)*8192+(r0+4)*128+((z^g0)*32)+p*8)); SEND SEND
        WAITL();
        if constexpr(c+2<5) csissue(c+2);
        GAP();
        SFOR(2,k) SFOR(4,z) if constexpr(c==0&&k==0) MFMA16Z(cs[z],fa[k][z],on); else MFMA16(cs[z],fa[k][z],on); SEND SEND
        GAP();
      SEND
      NOP8(); pin(cs[0],cs[1],cs[2],cs[3]);
      f32x4 v;
      #pragma unroll
      for(int e=0;e<4;e++) v[e]= gj==0? cs[0][e] : gj==1? cs[1][e] : gj==2? cs[2][e] : cs[3][e];
      const unsigned vo=(WS_CS+(kh*3456+cbk*64+16*gj+4*gq)*4)|((act&&gj<4)?0:OOB); ST4S(__builtin_bit_cast(u32x4,v),vo,rws,0,0);
    }
    STAMPM(3);
    BARRIER();
    conv_issue(ldsR0,4*spid+cvp,0); conv_issue(ldsR0,4*spid+cvp,1);
    conv_issue(ldsR1,4*spid+2+cvp,0); conv_issue(ldsR1,4*spid+2+cvp,1);
    conv_issue(ldsC,chl,0); conv_issue(ldsC,chl,1);
    waitv<24>(tk0); BARRIER();
    if(tid==0) put_flag(ws,WS_FQ+spid*8,tag);
    STAMPM(4);
    STAMPM(5);
  }
  u32x2 ph, xv[5], stv;
  const int trow=64*mt+16*w+gj;
  u32x4 H[14][4];
  unsigned vh[4];
  #pragma unroll
  for(int r=0;r<4;r++) vh[r]=WS_HF+((w+r)&3)*54*1024+lane*16;
  auto hoff=[&](int i) __attribute__((always_inline)) ->unsigned { return i<13? hbase+i*1024 : h13; };
  auto ldH=[&](int i) __attribute__((always_inline)) { const unsigned sh=hoff(i);
    LD4S(H[i][0],vh[0],rws,sh,0); LD4S(H[i][1],vh[1],rws,sh,0); LD4S(H[i][2],vh[2],rws,sh,0); LD4S(H[i][3],vh[3],rws,sh,0); };
  auto conv_round=[&](auto LASTC, const unsigned base, const int ch) __attribute__((always_inline)) {
    constexpr bool LAST=decltype(LASTC)::value;
    BARRIER();
    if(cvh==0){
      const int c=lane>>4,p=lane&15; const LAS u16* src=(const LAS u16*)(size_t)(base+32+ (p>=16-c? (p-16+c)*2:0));
      u16 v=*src; *(LAS u16*)(size_t)(base+c*CV_HC+p*2)= p>=16-c? v:(u16)0;
    } else if(lane<32) lw128(base+CV_H+4096+lane*16,u32x4{0,0,0,0});
    const float Dd=bflo(((const unsigned*)Dv)[ch>>1]>>((ch&1)*16));
    BARRIER();
    if constexpr(LAST){ STAMPL(6); }
    const int ci=gj,cq=gq,cc=(ci+1)&3;
    const unsigned pa=opq(base+cc*CV_HC+2*(1+ci-cc+8*cq)+256*cvh), pb=opq(base+CV_H+2*(2032-16*ci+8*cq)-3584+256*cvh);
    f32x4 ya[8];
    { u32x4 B[2][8], A[2][8];
      auto ldA=[&](int rho,int a)->u32x4{ const unsigned p=pa+64*rho+512*a; return cat2(lr64v(p),lr64v(p+8)); };
      auto ldB=[&](int rho,int bb)->u32x4{ return lr128(pb+64*rho+512*(7-bb)); };
      #pragma unroll
      for(int z=0;z<8;z++){ B[0][z]=ldB(0,z); A[0][z]=ldA(0,z); }
      GAP();
      SFOR(4,rho)
        constexpr int c=rho&1,n=c^1;
        if constexpr(LAST && rho==2){
          WAITV(15); if(isg && lane==0) put_flag(ws,WS_FH+(mi*64+ni*4+w)*8,tag); }
        if constexpr(LAST && rho==3){ const unsigned vo=(WS_FH+((mt>>1)*64+lane)*8)|(lane<56?0:OOB); LD2S(ph,vo,rws,0,0); }
        SFOR(8,a)
          SFOR(8-a,nn_)
            constexpr int nu=a+nn_; constexpr int cnt=a*8-(a*(a-1))/2+(nu-a);
            if constexpr(rho==0 && a==0) MFMA16Z(ya[nu],A[c][a],B[c][nu-a]); else MFMA16(ya[nu],A[c][a],B[c][nu-a]);
            if constexpr(rho<3){
              if constexpr(cnt<8) B[n][cnt]=ldB(rho+1,cnt);
              else if constexpr(cnt<24){ constexpr int az=(cnt-8)>>1; const u32x2 t=lr64v(pa+64*(rho+1)+512*az+8*(cnt&1));
                if constexpr((cnt&1)==0){ u32x4 v={t[0],t[1],t[0],t[1]}; A[n][az]=v; } else { A[n][az][2]=t[0]; A[n][az][3]=t[1]; } } }
            if constexpr(LAST && rho<3 && cnt>=24 && cnt<31){ constexpr int nn=NWE+rho*7+(cnt-24); if constexpr(nn<64) ldwd(IC(nn)); }
            if constexpr(LAST && rho==3 && cnt>=4 && cnt<28 && cnt%4==0){
              constexpr int q=cnt/4-1; const unsigned vo=(trow*D_+80*nt+4*gq)*2;
              if constexpr(q<5) ld2<32*q>(xv[q],vo,rx,0); else { const unsigned vs=WS_ST+trow*8; LD2S(stv,vs,rws,0,0); } }
            GAP();
          SEND
        SEND
      SEND
    }
    if constexpr(LAST){ STAMPL(7);
      waitv<6>(ph);
      if(!__all((ph[0]==(unsigned)tag && ph[1]==(unsigned)(tag>>32)) || lane>=56)) spin4(rws,WS_FH+(mt>>1)*512,14,tag);
      ldH(0); ldH(1); ldH(2); ldH(3); }
    NOP8();
    f32x4 mine[4];
    { const unsigned R=lds0+L_RED+w*4096+lane*16;
      #pragma unroll
      for(int z=0;z<4;z++){ const f32x4 lo=ya[z], hi=ya[4+z]; f32x4 snd, kp;
        #pragma unroll
        for(int e=0;e<4;e++){ snd[e]= cvh? lo[e]:hi[e]; kp[e]= cvh? hi[e]:lo[e]; }
        lw128f(R+z*1024,snd); mine[z]=kp; } }
    BARRIER();
    { const unsigned R=lds0+L_RED+(w^1)*4096+lane*16; unsigned pk[4][2];
      #pragma unroll
      for(int z=0;z<4;z++){ const f32x4 y=mine[z]+lr128f(R+z*1024);
        const int t=256*(4*cvh+z)+16*ci+4*cq;
        const u32x2 uu=*(const LAS u32x2*)(size_t)(base+CV_H+2*(2044-t));
        pk[z][0]=cvtpk(fmaf(bfhi(uu[1]),Dd,y[0]),fmaf(bflo(uu[1]),Dd,y[1])); pk[z][1]=cvtpk(fmaf(bfhi(uu[0]),Dd,y[2]),fmaf(bflo(uu[0]),Dd,y[3])); }
      #pragma unroll
      for(int z=0;z<4;z+=2){
        auto r0=__builtin_amdgcn_permlane16_swap(pk[z][0],pk[z+1][0],false,false);
        auto r1=__builtin_amdgcn_permlane16_swap(pk[z][1],pk[z+1][1],false,false);
        u32x4 v={r0[0],r1[0],r0[1],r1[1]};
        const int t=256*(4*cvh+z+(cq&1))+16*ci+8*(cq>>1);
        const unsigned vo=WS_YT+ch*4096+t*2; ST4S(v,vo,rws,0,0); }
    }
  };
  if(!isg){
    spin4(rws,WS_FP,64,tag);
    conv_issue(ldsR0,4*spid+cvp,2); conv_issue(ldsR1,4*spid+2+cvp,2); conv_issue(ldsC,chl,2);
    unsigned tw[9];
    SFOR(9,i) const unsigned li=((jj-28)*4+w)*540+i*64+lane; const unsigned vt=(WS_WDF+(xcd&1)*4*276480+li*128)|((i*64+lane<540)?0:OOB); ld1s<0>(tw[i],vt,rws,0); SEND
    WAITV(13);
    STAMP4(6);
    conv_round(std::integral_constant<bool,false>{},ldsR0,4*spid+cvp);
    STAMP4(7);
    WAITV(13);
    conv_round(std::integral_constant<bool,false>{},ldsR1,4*spid+2+cvp);
    STAMP4(8);
    waitv<2>(tw[0],tw[1],tw[2],tw[3],tw[4],tw[5],tw[6],tw[7],tw[8]);
    if(lane==0) put_flag(ws,WS_FY+((4*spid+cvp)*2+cvh)*8,tag);
    spin4(rws,WS_FQ,8,tag);
    if(lane==0) put_flag(ws,WS_FY+((4*spid+2+cvp)*2+cvh)*8,tag);
    STAMP4(9);
    SFOR(NWE,n) ldwd(IC(n)); SEND
    STAMP4(10);
  }
  conv_round(std::integral_constant<bool,true>{},ldsC,chl);
  STAMPL(8);
  u32x4 WV[6]; u32x4 py0,py1; u32x4 yv[3];
  f32x4 oa[4][5];
  #define HREGS(i) H[i][0],H[i][1],H[i][2],H[i][3]
  SFOR(14,i)
    if constexpr(i==0){ waitv<14>(HREGS(0),xv[0],xv[1],xv[2],xv[3],xv[4],stv); }
    if constexpr(i==1){ waitv<14>(HREGS(1)); STAMP2(1);
                        }
    if constexpr(i==2) waitv<14>(HREGS(2));
    if constexpr(i==3){ waitv<12>(HREGS(3)); STAMP2(2);
                        if(lane==0) put_flag(ws,WS_FY+(chl*2+cvh)*8,tag); }
    if constexpr(i==4) waitv<12>(HREGS(4));
    if constexpr(i==5) waitv<18>(HREGS(5));
    if constexpr(i==6) waitv<20>(HREGS(6));
    if constexpr(i==7){ waitv<20>(HREGS(7)); STAMP2(3); }
    if constexpr(i==8) waitv<14>(HREGS(8),WV[0],WV[1],WV[2],WV[3],WV[4],WV[5]);
    if constexpr(i==9){ waitv<12>(HREGS(9),py0,py1); STAMP2(4);
              if(!__all((tag4(py0,tag)&&tag4(py1,tag))||lane>=40)){
                spin4(rws,WS_FY+160*nt*8,40,tag); } }
    if constexpr(i==10){ waitv<12>(HREGS(10)); STAMP2(5); }
    if constexpr(i==11) waitv<11>(HREGS(11));
    if constexpr(i==12) waitv<7>(HREGS(12));
    if constexpr(i==13) waitv<3>(HREGS(13));
    GAP();
    SFOR(4,tt)
      SFOR(5,dt)
        constexpr int nn=5*i+dt, sl=tt*5+dt;
        if constexpr(i==0) mfmawz<nn>(oa[tt][dt],H[0][tt]);
        else if constexpr(nn<64) mfmaw<nn>(oa[tt][dt],H[i][tt]);
        else MFMA16(oa[tt][dt],WV[nn-64],H[i][tt]);
        if constexpr(i+4<14 && sl%4==1 && sl<16){ const unsigned sh=hoff(i+4); ld4s<0>(H[i+4][sl>>2],vh[sl>>2],rws,sh); }
        if constexpr(i==4 && sl%3==0 && sl>=2 && sl<20){ constexpr int z=sl/3-1; if constexpr(z<6){ constexpr int n2=64+z, i2=n2/5, d2=n2%5;
            const unsigned so=(i2<13? wdbase+i2*5120 : wd13)+d2*1024; ld4s<0>(WV[z],vw0,rws,so); } }
        if constexpr(i==5 && sl==17){ const unsigned vo=(WS_FY+(160*nt+4*lane)*8)|(lane<40?0:OOB); LD4S(py0,vo,rws,0,0); LD4S(py1,vo,rws,0,16); }
        if constexpr(i==10 && sl%6==2){ constexpr int z=sl/6; const int id=tid+256*z; const int r=id>>3,c8=id&7;
            const unsigned vo=(WS_YT+(80*nt+r)*4096+(64*mt+c8*8)*2)|(id<640?0:OOB); LD4S(yv[z],vo,rws,0,0); }
        GAP();
      SEND
    SEND
  SEND
  STAMPL(9);
  waitv<0>(yv[0],yv[1],yv[2]);
  #pragma unroll
  for(int z=0;z<3;z++){ const int id=tid+256*z; if(id<640) lw128(lds0+L_Y+(id>>3)*160+(id&7)*16,yv[z]); }
  BARRIER();
  NOP8();
  #pragma unroll
  for(int r=1;r<4;r++){ const unsigned a=lds0+((((w+r)&3)*3+(r-1))*5)*1024+lane*16;
    #pragma unroll
    for(int dt=0;dt<5;dt++) lw128f(a+dt*1024,oa[r][dt]); }
  BARRIER();
  STAMPL(10);
  { const float m=__uint_as_float(stv[0]), rd=__uint_as_float(stv[1]); const unsigned a=lds0+(w*15)*1024+lane*16;
    #pragma unroll
    for(int dt=0;dt<5;dt++){
      f32x4 f=(oa[0][dt]+lr128f(a+dt*1024))+(lr128f(a+(5+dt)*1024)+lr128f(a+(10+dt)*1024));
      const u32x2 y2=__builtin_bit_cast(u32x2,trread(lds0+L_Y+(16*dt+4*gq+((lane&15)>>2))*160+(16*w+4*(lane&3))*2));
      const float xf[4]={bflo(xv[dt][0]),bfhi(xv[dt][0]),bflo(xv[dt][1]),bfhi(xv[dt][1])};
      const float yf[4]={bflo(y2[0]),bfhi(y2[0]),bflo(y2[1]),bfhi(y2[1])};
      float o[4];
      #pragma unroll
      for(int e=0;e<4;e++) o[e]=fmaf((xf[e]-m)*rd,yf[e],xf[e])+f[e];
      u32x2 ov={cvtpk(o[0],o[1]),cvtpk(o[2],o[3])};
      *(u32x2*)(out+(size_t)trow*D_+80*nt+16*dt+4*gq)=ov;
    } }
  STAMPL(11);
  if(b==XWG){ u64 mn=~0ull,mx=0;
    for(int i=0;i<4;i++){ u64 v=__hip_atomic_load((GAS u64*)(ws+(40u<<20))+i*64+lane,__ATOMIC_RELAXED,__HIP_MEMORY_SCOPE_AGENT); mn=v<mn?v:mn; mx=v>mx?v:mx; }
    for(int o=1;o<64;o<<=1){ u64 a=__shfl_xor(mn,o), c=__shfl_xor(mx,o); mn=a<mn?a:mn; mx=c>mx?c:mx; }
    const u64 te=__builtin_amdgcn_s_memrealtime();
    const u64 xt0=__hip_atomic_load((GAS u64*)(ws+(40u<<20))+XWG,__ATOMIC_RELAXED,__HIP_MEMORY_SCOPE_AGENT);
    u64 d= XMODE==1? mx-mn : XMODE==2? xt0-mn : te-mn; d*=4; if(d>40000) d=40000;
    while(__builtin_amdgcn_s_memrealtime()-te<d) __builtin_amdgcn_s_sleep(1); }
}

extern "C" void kernel_launch(void* const* d_in, const int* in_sizes, int n_in, void* d_out, int out_size, void* d_ws, size_t ws_size, hipStream_t stream){
  (void)hipFuncSetAttribute((const void*)fused_kernel,hipFuncAttributeMaxDynamicSharedMemorySize,LDS_BYTES);
  fused_kernel<<<256,256,LDS_BYTES,stream>>>((const u16*)d_in[0],(const u16*)d_in[1],(const u16*)d_in[2],(const u16*)d_in[3],(const u16*)d_in[4],(const u16*)d_in[5],
      (const u16*)d_in[7],(const u16*)d_in[8],(const u16*)d_in[9],(u16*)d_out,(char*)d_ws);
}
```

```cpp
#include <hip/hip_runtime.h>
#include <hip/hip_bf16.h>
#include <stdint.h>
#include <type_traits>
#include <utility>
#define LAS __attribute__((address_space(3)))
#define GAS __attribute__((address_space(1)))
#define DI __device__ __forceinline__
typedef unsigned short u16;
typedef unsigned long long u64;
typedef short s16x4 __attribute__((ext_vector_type(4)));
typedef short s16x8 __attribute__((ext_vector_type(8)));
typedef __bf16 bf16x2 __attribute__((ext_vector_type(2)));
typedef float f32x2 __attribute__((ext_vector_type(2)));
typedef float f32x4 __attribute__((ext_vector_type(4)));
typedef unsigned u32x4 __attribute__((ext_vector_type(4)));
typedef unsigned u32x2 __attribute__((ext_vector_type(2)));
constexpr int S_=2048, D_=640, F_=1728;
#define ROT 0
#define NTX 0
#define NTSX 0
#define NTSW 0
#define NTCS 0
#define TKM 0
#define PORD 0
#define GAP() __builtin_amdgcn_sched_barrier(0)
DI unsigned rfl(unsigned x){ return __builtin_amdgcn_readfirstlane(x); }
DI u32x4 mk_rsrc(const void* p, unsigned bytes){
  unsigned long long a=(unsigned long long)p;
  u32x4 r; r[0]=rfl((unsigned)a); r[1]=rfl((unsigned)(a>>32))&0xffffu; r[2]=bytes; r[3]=0x00020000u; return r;
}
DI void setm0(unsigned x){ asm volatile("s_mov_b32 m0, %0\n\ts_nop 0"::"s"(x)); }
template<int I> DI void dma (unsigned vo,const u32x4& r,unsigned so){ asm volatile("buffer_load_dwordx4 %0, %1, %2 offen offset:%3 lds"::"v"(vo),"s"(r),"s"(so),"n"(I)); }
template<int I> DI void dmas(unsigned vo,const u32x4& r,unsigned so){ asm volatile("buffer_load_dwordx4 %0, %1, %2 offen offset:%3 sc1 lds"::"v"(vo),"s"(r),"s"(so),"n"(I)); }
template<int I> DI void ld4  (u32x4& d,unsigned vo,const u32x4& r,unsigned so){ asm volatile("buffer_load_dwordx4 %0, %1, %2, %3 offen offset:%4":"=v"(d):"v"(vo),"s"(r),"s"(so),"n"(I)); }
template<int I> DI void dman(unsigned vo,const u32x4& r,unsigned so){ asm volatile("buffer_load_dwordx4 %0, %1, %2 offen offset:%3 nt lds"::"v"(vo),"s"(r),"s"(so),"n"(I)); }
template<int I> DI void ld4n (u32x4& d,unsigned vo,const u32x4& r,unsigned so){ asm volatile("buffer_load_dwordx4 %0, %1, %2, %3 offen offset:%4 nt":"=v"(d):"v"(vo),"s"(r),"s"(so),"n"(I)); }
template<int N,int I> DI void ldwa(unsigned vo,const u32x4& r,unsigned so){ asm volatile("buffer_load_dwordx4 a[%c3:%c4], %0, %1, %2 offen offset:%5 sc1"::"v"(vo),"s"(r),"s"(so),"n"(4*N),"n"(4*N+3),"n"(I)); }
template<int N> DI void mfmaw (f32x4& acc,const u32x4& b){ asm volatile("v_mfma_f32_16x16x32_bf16 %0, a[%c2:%c3], %1, %0":"+v"(acc):"v"(b),"n"(4*N),"n"(4*N+3)); }
template<int N> DI void mfmawz(f32x4& acc,const u32x4& b){ asm volatile("v_mfma_f32_16x16x32_bf16 %0, a[%c2:%c3], %1, 0":"=&v"(acc):"v"(b),"n"(4*N),"n"(4*N+3)); }
template<int NT,int I> DI void dmap(unsigned vo,const u32x4& r,unsigned so){ if constexpr(NT) dman<I>(vo,r,so); else dma<I>(vo,r,so); }
template<int NT,int I> DI void ld4p(u32x4& d,unsigned vo,const u32x4& r,unsigned so){ if constexpr(NT) ld4n<I>(d,vo,r,so); else ld4<I>(d,vo,r,so); }
template<int I> DI void ld1n (unsigned& d,unsigned vo,const u32x4& r,unsigned so){ asm volatile("buffer_load_dword %0, %1, %2, %3 offen offset:%4 nt":"=v"(d):"v"(vo),"s"(r),"s"(so),"n"(I)); }
template<int I> DI void ld4s (u32x4& d,unsigned vo,const u32x4& r,unsigned so){ asm volatile("buffer_load_dwordx4 %0, %1, %2, %3 offen offset:%4 sc1":"=v"(d):"v"(vo),"s"(r),"s"(so),"n"(I)); }
template<int I> DI void ld4sa(u32x4& d,unsigned vo,const u32x4& r,unsigned so){ asm volatile("buffer_load_dwordx4 %0, %1, %2, %3 offen offset:%4 sc1":"=a"(d):"v"(vo),"s"(r),"s"(so),"n"(I)); }
template<int I> DI void ld2s (u32x2& d,unsigned vo,const u32x4& r,unsigned so){ asm volatile("buffer_load_dwordx2 %0, %1, %2, %3 offen offset:%4 sc1":"=v"(d):"v"(vo),"s"(r),"s"(so),"n"(I)); }
template<int I> DI void ld2  (u32x2& d,unsigned vo,const u32x4& r,unsigned so){ asm volatile("buffer_load_dwordx2 %0, %1, %2, %3 offen offset:%4":"=v"(d):"v"(vo),"s"(r),"s"(so),"n"(I)); }
template<int I> DI void ld1s (unsigned& d,unsigned vo,const u32x4& r,unsigned so){ asm volatile("buffer_load_dword %0, %1, %2, %3 offen offset:%4 sc1":"=v"(d):"v"(vo),"s"(r),"s"(so),"n"(I)); }
template<int I> DI void ld1  (unsigned& d,unsigned vo,const u32x4& r,unsigned so){ asm volatile("buffer_load_dword %0, %1, %2, %3 offen offset:%4":"=v"(d):"v"(vo),"s"(r),"s"(so),"n"(I)); }
template<int I> DI void st4s (const u32x4& d,unsigned vo,const u32x4& r,unsigned so){ asm volatile("buffer_store_dwordx4 %0, %1, %2, %3 offen offset:%4 sc1\n\ts_nop 1"::"v"(d),"v"(vo),"s"(r),"s"(so),"n"(I)); }
template<int I> DI void st2s (const u32x2& d,unsigned vo,const u32x4& r,unsigned so){ asm volatile("buffer_store_dwordx2 %0, %1, %2, %3 offen offset:%4 sc1\n\ts_nop 1"::"v"(d),"v"(vo),"s"(r),"s"(so),"n"(I)); }
#define SETM0(x) setm0(x)
#define DMA(voff,rsrc,soff,imm) dma<imm>(voff,rsrc,soff)
#define DMAS(voff,rsrc,soff,imm) dmas<imm>(voff,rsrc,soff)
#define LD4(dst,voff,rsrc,soff,imm) ld4<imm>(dst,voff,rsrc,soff)
#define LD4S(dst,voff,rsrc,soff,imm) ld4s<imm>(dst,voff,rsrc,soff)
#define LD4SA(dst,voff,rsrc,soff,imm) ld4sa<imm>(dst,voff,rsrc,soff)
#define LD2S(dst,voff,rsrc,soff,imm) ld2s<imm>(dst,voff,rsrc,soff)
#define LD2(dst,voff,rsrc,soff,imm) ld2<imm>(dst,voff,rsrc,soff)
#define LD1S(dst,voff,rsrc,soff,imm) ld1s<imm>(dst,voff,rsrc,soff)
#define LD1(dst,voff,rsrc,soff,imm) ld1<imm>(dst,voff,rsrc,soff)
#define ST4S(src,voff,rsrc,soff,imm) st4s<imm>(src,voff,rsrc,soff)
#define ST2S(src,voff,rsrc,soff,imm) st2s<imm>(src,voff,rsrc,soff)
template<int N> DI void waitv(){ asm volatile("s_waitcnt vmcnt(%0)"::"n"(N):"memory"); }
template<class T> DI void pin(T& a){ asm volatile("":"+v"(a)::"memory"); }
template<class T,class... R> DI void pin(T& a,R&... r){ pin(a); pin(r...); }
template<int N,class... R> DI void waitv(R&... r){ asm volatile("s_waitcnt vmcnt(%0)"::"n"(N):"memory"); pin(r...); }
#define WAITV(n) waitv<n>()
#define WAITL() asm volatile("s_waitcnt lgkmcnt(0)":::"memory")
#define BARRIER() asm volatile("s_waitcnt lgkmcnt(0)\n\ts_barrier":::"memory")
DI void MFMA16  (f32x4& acc,const u32x4& a,const u32x4& b){ asm volatile("v_mfma_f32_16x16x32_bf16 %0, %1, %2, %0":"+v"(acc):"v"(a),"v"(b)); }
DI void MFMA16Z (f32x4& acc,const u32x4& a,const u32x4& b){ asm volatile("v_mfma_f32_16x16x32_bf16 %0, %1, %2, 0":"=&v"(acc):"v"(a),"v"(b)); }
DI void MFMA16A (f32x4& acc,const u32x4& a,const u32x4& b){ asm volatile("v_mfma_f32_16x16x32_bf16 %0, %1, %2, %0":"+v"(acc):"a"(a),"v"(b)); }
DI void MFMA16AZ(f32x4& acc,const u32x4& a,const u32x4& b){ asm volatile("v_mfma_f32_16x16x32_bf16 %0, %1, %2, 0":"=&v"(acc):"a"(a),"v"(b)); }
DI void NOP8(){ asm volatile("s_nop 7":::"memory"); __builtin_amdgcn_sched_barrier(0); }
DI float bflo(unsigned x){ return __uint_as_float(x<<16); }
DI float bfhi(unsigned x){ return __uint_as_float(x&0xffff0000u); }
DI unsigned cvtpk(float lo,float hi){ f32x2 v={lo,hi}; bf16x2 b=__builtin_convertvector(v,bf16x2); return __builtin_bit_cast(unsigned,b); }
DI float rbf(float x){ return bflo(cvtpk(x,x)); }
DI u32x4 cat(s16x4 a, s16x4 b){ s16x8 r=__builtin_shufflevector(a,b,0,1,2,3,4,5,6,7); return __builtin_bit_cast(u32x4,r); }
DI u32x4 cat2(u32x2 a,u32x2 b){ return __builtin_shufflevector(a,b,0,1,2,3); }
DI s16x4 trread(unsigned a){ return __builtin_amdgcn_ds_read_tr16_b64_v4i16((LAS s16x4*)(size_t)a); }
DI u32x4 lr128(unsigned a){ return *(const LAS u32x4*)(size_t)a; }
DI f32x4 lr128f(unsigned a){ return *(const LAS f32x4*)(size_t)a; }
DI u32x2 lr64v(unsigned a){ return *(const volatile LAS u32x2*)(size_t)a; }
DI void lw128(unsigned a,u32x4 v){ *(LAS u32x4*)(size_t)a=v; }
DI void lw128f(unsigned a,f32x4 v){ *(LAS f32x4*)(size_t)a=v; }
DI void lw64(unsigned a,u32x2 v){ *(LAS u32x2*)(size_t)a=v; }
DI unsigned opq(unsigned x){ asm volatile("":"+v"(x)); return x; }
template<int CTRL> DI float dpp_add(float v){ return v+__builtin_bit_cast(float,__builtin_amdgcn_update_dpp(0,__builtin_bit_cast(int,v),CTRL,0xf,0xf,true)); }
DI float sum32(float v){
  v=dpp_add<0x128>(v); v=dpp_add<0x124>(v); v=dpp_add<0x122>(v); v=dpp_add<0x121>(v);
  auto r=__builtin_amdgcn_permlane16_swap(__float_as_uint(v),__float_as_uint(v),false,false);
  return __uint_as_float(r[0])+__uint_as_float(r[1]); }

constexpr unsigned OOB=0xfff00000u;
template<int... I,class F> DI void sfor_impl(std::integer_sequence<int,I...>,F&& f){ (f(std::integral_constant<int,I>{}),...); }
template<int N,class F> DI void sfor(F&& f){ sfor_impl(std::make_integer_sequence<int,N>{},f); }
#define SFOR(N,v) sfor<N>([&](auto v##_) __attribute__((always_inline)) { constexpr int v=v##_;
#define SEND });
#define IC(v) std::integral_constant<int,(v)>{}
constexpr unsigned WS_E=0, WS_FP=256, WS_FQ=WS_FP+2048, WS_FH=WS_FQ+256, WS_FY=WS_FH+8192, WS_FLAGS_END=WS_FY+10240;
constexpr unsigned WS_ST=24576  , WS_CS=WS_ST+16384  , WS_UT=WS_CS+57344, WS_WDF=WS_UT+D_*S_*2, WS_HF=WS_WDF+F_*D_*2, WS_YT=WS_HF+S_*F_*2, WS_END=WS_YT+D_*S_*2;
constexpr unsigned WS_DBG=32u<<20;
static_assert(WS_FLAGS_END<=WS_ST,"");
constexpr int STG=49152;
constexpr int L_KV=2*STG, L_CSD=2*STG+16384;
constexpr int L_TAB=3*STG;
constexpr int CV_HC=4160, CV_H=4*CV_HC, CV_U=4608, CV_CH=CV_H+CV_U;
constexpr int L_CV=STG;
constexpr int L_RED=2*STG;
constexpr int L_Y=L_TAB;
constexpr int LDS_BYTES=163840;

#define STAMP(i)

#define STAMP2(i)
#define STAMPM(i) STAMP(i)
#define STAMP5(i)
#define STAMP4(i)
#define STAMP3(i)
#define STAMPL(i) STAMP(i)
DI void spin4(const u32x4& rws, unsigned off, int nlanes, u64 tag){
  const int lane=threadIdx.x&63; const unsigned vo=(off+lane*32)|(lane<nlanes?0:OOB); const unsigned tl=(unsigned)tag, th=(unsigned)(tag>>32);
  const u64 t0=__builtin_amdgcn_s_memrealtime();
  for(;;){
    u32x4 a,c; ld4s<0>(a,vo,rws,0); ld4s<16>(c,vo,rws,0); waitv<0>(a,c);
    const bool ok= a[0]==tl&&a[1]==th&&a[2]==tl&&a[3]==th&&c[0]==tl&&c[1]==th&&c[2]==tl&&c[3]==th;
    if(__all(ok || lane>=nlanes)) break;
    if(__builtin_amdgcn_s_memrealtime()-t0>2000000ull) break;
    __builtin_amdgcn_s_sleep(1);
  }
}
DI void put_flag(char* ws, unsigned off, u64 tag){ __hip_atomic_store((GAS u64*)(ws+off),tag,__ATOMIC_RELAXED,__HIP_MEMORY_SCOPE_AGENT); }
DI bool tag4(u32x4 v,u64 tag){ const unsigned tl=(unsigned)tag, th=(unsigned)(tag>>32); return v[0]==tl&&v[1]==th&&v[2]==tl&&v[3]==th; }

__global__ __launch_bounds__(256,1) void fused_kernel(const u16* __restrict__ x,const u16* __restrict__ kk,const u16* __restrict__ vv,const u16* __restrict__ sk,
    const u16* __restrict__ hk,const u16* __restrict__ Dv,const u16* __restrict__ wg,const u16* __restrict__ wu,const u16* __restrict__ wd,u16* __restrict__ out,char* ws){
  extern __shared__ __attribute__((aligned(16))) char smem[];
  STAMP(0);
  asm volatile("":::"a0","a255");
  asm volatile(""::"s"(x),"s"(kk),"s"(vv),"s"(sk),"s"(hk),"s"(Dv),"s"(wg),"s"(wu),"s"(wd),"s"(out),"s"(ws));
  const int tid=threadIdx.x, lane=tid&63; const unsigned w=rfl(tid>>6); const int b=blockIdx.x;
  const unsigned lds0=(unsigned)(size_t)(LAS char*)smem;
  const u32x4 rws=mk_rsrc(ws,WS_END), rx=mk_rsrc(x,S_*D_*2);
  u32x2 Eraw; { const unsigned z=0; LD2S(Eraw,z,rws,0,0); }
  const int lrow=8*b+2*w+(lane>>5), l32=lane&31;
  u32x4 xc3[3];
  { const unsigned vo=lrow*1280+l32*16; ld4p<NTX,0>(xc3[0],vo,rx,0); ld4p<NTX,512>(xc3[1],vo,rx,0); const unsigned v2=(vo+1024)|(l32<16?0:OOB); ld4p<NTX,0>(xc3[2],v2,rx,0); }
  STAMP3(6);
  constexpr int NPF1=0;
  constexpr int NPF=NPF1;
  auto pinpf=[&]() __attribute__((always_inline)) {
  };
  const int xcd=b&7, jj=b>>3; const bool isg=jj<28;
  const int g1id=xcd*28+jj, spid=xcd*4+jj-28;
  const int mi=(xcd>>1)*4+(jj&3), ni=(xcd&1)*7+(jj>>2);
  const int gj=lane&15,gq=lane>>4;
  const bool iskv=isg && g1id>=64; const int kvt=g1id-64;
  const int tb=kvt/5, cb=kvt-5*tb, t0=tb*64, pc_=tid&15, tg=tid>>4, d0=cb*128+pc_*8;
  STAMP3(7);
  const u32x4 rg=mk_rsrc(wg,D_*F_*2), ru=mk_rsrc(wu,D_*F_*2);
  unsigned vx[4], vw[4];
  #pragma unroll
  for(int i=0;i<4;i++){ const int row=32*w+8*i+(lane>>3); const int c=(lane&7)^((row>>1)&7); vx[i]=(128*mi+row)*1280+c*16-1024*i;
    const int k=16*w+4*i+(lane>>4), cp=lane&15, gs=(cp>>1)^((k&3)|(((k>>3)&1)<<2)); vw[i]=k*3456+ni*256+gs*32+(cp&1)*16-1024*i; }
  const unsigned m0x=rfl(lds0+w*4096), m0g=m0x+16384, m0u=m0x+32768;
  auto piece=[&](int P,int st,int bf) __attribute__((always_inline)) {
    if(P==0) SETM0(m0x+bf*STG); if(P==4) SETM0(m0g+bf*STG); if(P==8) SETM0(m0u+bf*STG);
    const unsigned sx=st*128, sw=st*221184;
    if(P==0) dmap<NTSX,0>(vx[0],rx,sx); if(P==1) dmap<NTSX,1024>(vx[1],rx,sx); if(P==2) dmap<NTSX,2048>(vx[2],rx,sx); if(P==3) dmap<NTSX,3072>(vx[3],rx,sx);
    if(P==4) dmap<NTSW,0>(vw[0],rg,sw); if(P==5) dmap<NTSW,1024>(vw[1],rg,sw); if(P==6) dmap<NTSW,2048>(vw[2],rg,sw); if(P==7) dmap<NTSW,3072>(vw[3],rg,sw);
    if(P==8) dmap<NTSW,0>(vw[0],ru,sw); if(P==9) dmap<NTSW,1024>(vw[1],ru,sw); if(P==10) dmap<NTSW,2048>(vw[2],ru,sw); if(P==11) dmap<NTSW,3072>(vw[3],ru,sw);
  };
  u64 tag;
  auto stats=[&]() __attribute__((always_inline)) {
  tag=((u64)rfl(Eraw[0])|((u64)rfl(Eraw[1])<<32))*6364136223846793005ull+1442695040888963407ull;
  {
    f32x2 f[12];
    #pragma unroll
    for(int i=0;i<3;i++) for(int e=0;e<4;e++) f[4*i+e]=f32x2{bflo(xc3[i][e]),bfhi(xc3[i][e])};
    f32x2 s0=(f[0]+f[1])+(f[2]+f[3]), s1=(f[4]+f[5])+(f[6]+f[7]), s2=(f[8]+f[9])+(f[10]+f[11]); s0=(s0+s1)+s2;
    const float mean=sum32(s0[0]+s0[1])*(1.f/640.f);
    f32x2 q0={0.f,0.f},q1={0.f,0.f},q2={0.f,0.f};
    #pragma unroll
    for(int i=0;i<4;i++){ f32x2 d=f[i]-mean, e=f[4+i]-mean, g=f[8+i]-mean; q0=d*d+q0; q1=e*e+q1; q2=g*g+q2; }
    q0+=q1;
    const float var=rbf(sum32(q0[0]+q0[1]+(l32<16? q2[0]+q2[1]:0.f))*(1.f/640.f)); const float m=rbf(mean);
    const float den=rbf(__builtin_amdgcn_sqrtf(rbf(var+1e-5f)));
    u32x2 sv={__float_as_uint(m),__float_as_uint(__builtin_amdgcn_rcpf(den))}; const unsigned vo=(WS_ST+lrow*8)|(l32==0?0:OOB);
    ST2S(sv,vo,rws,0,0);
  }
  };
  const int mt=(xcd>>1)*8+(jj&7), nt=(xcd&1)*4+(jj>>3);
  const u32x4 rhk=mk_rsrc(hk,D_*S_*2);
  const int cvp=w>>1, cvh=w&1;
  const unsigned ldsC=rfl(lds0+L_CV+cvp*CV_CH);
  const int chl=128+2*b+cvp;
  const unsigned vw0=lane*16, vw1=vw0+4096;
  const int kst=14*w-(w==3?1:0);
  const unsigned wdbase=rfl(WS_WDF+(nt*54+kst)*5120), hbase=rfl((4*mt*54+kst)*1024);
  const unsigned wd13=rfl(w<2? wdbase+13*5120 : 0x10000000u), h13=rfl(w<2? hbase+13*1024 : 0x10000000u);
  auto ldwd=[&](auto NC) __attribute__((always_inline)) { constexpr int n=decltype(NC)::value, i=n/5, dt=n%5; static_assert(i<13,"");
    const unsigned so=wdbase+i*5120; if constexpr(dt<4) ldwa<n,dt*1024>(vw0,rws,so); else ldwa<n,0>(vw1,rws,so); };
  auto conv_issue=[&](unsigned base,int ch,int part) __attribute__((always_inline)) {
    unsigned so=rfl(ch*4096); asm volatile("s_nop 4":"+s"(so));
    if(part<2){ const int c=2*cvh+part; const unsigned vo=lane*16+2*c; SETM0(base+c*CV_HC+32); dman<0>(vo,rhk,so); dman<1024>(vo,rhk,so); dman<2048>(vo,rhk,so); dman<3072>(vo,rhk,so); }
    else { const unsigned vo=WS_UT+lane*16+cvh*2048; SETM0(base+CV_H+cvh*2048); DMAS(vo,rws,so,0); DMAS(vo,rws,so,1024); }
  };
  const unsigned ldsR0=rfl(lds0+cvp*CV_CH), ldsR1=rfl(lds0+2*STG+16384+cvp*CV_CH);
  constexpr int NWE=44;
  if(isg){
    auto middle=[&](auto NLC, unsigned& tk) __attribute__((always_inline)) { constexpr int NL=decltype(NLC)::value;
      { const unsigned vo=((128+2*b)*4096+lane*128)|((w==0&&TKM)?0:OOB); if constexpr(TKM==2) ld1n<0>(tk,vo,rhk,0); else LD1(tk,vo,rhk,0,0); }
      waitv<NL+1+NPF>(Eraw,xc3[0],xc3[1],xc3[2]);
      STAMP3(8);
      stats();
      STAMPM(1);
      #pragma unroll
      for(int P=0;P<12;P++) piece(P,0,0); };
    const unsigned P=lds0+L_KV;
    if(iskv){
      u32x4 pr[15];
      const u32x4 rk=mk_rsrc(kk,S_*D_*2), rv=mk_rsrc(vv,S_*D_*2), rsk=mk_rsrc(sk,D_*8);
      const unsigned vb=((t0+4*tg)*D_+d0)*2, va=(vb-3840)|((t0+tg==0)?OOB:0), vs=d0*8;
      ld4n<0>(pr[0],va,rk,0); ld4n<1280>(pr[1],va,rk,0); ld4n<2560>(pr[2],va,rk,0);
      ld4n<0>(pr[3],vb,rk,0); ld4n<1280>(pr[4],vb,rk,0); ld4n<2560>(pr[5],vb,rk,0); ld4n<3840>(pr[6],vb,rk,0);
      ld4n<0>(pr[7],vb,rv,0); ld4n<1280>(pr[8],vb,rv,0); ld4n<2560>(pr[9],vb,rv,0); ld4n<3840>(pr[10],vb,rv,0);
      ld4<0>(pr[11],vs,rsk,0); ld4<16>(pr[12],vs,rsk,0); ld4<32>(pr[13],vs,rsk,0); ld4<48>(pr[14],vs,rsk,0);
      unsigned vu=opq(WS_UT+(cb*128+(tid>>3))*4096+(S_-64-t0+(tid&7)*8)*2), lw_=opq(P+8*pc_*128+((15-tg)^pc_)*8);
      unsigned tk; middle(IC(15),tk);
      waitv<13>(pr[0],pr[1],pr[2],pr[3],pr[4],pr[5],pr[6],pr[7],pr[8],pr[9],pr[10],pr[11],pr[12],pr[13],pr[14],tk);
      pinpf();
      STAMP3(9);
      #pragma unroll
      for(int e2=0;e2<4;e2++){
        piece(3*e2,1,1); piece(3*e2+1,1,1); piece(3*e2+2,1,1);
        const f32x2 w0={bflo(pr[11+e2][0]),bflo(pr[11+e2][2])}, w1={bfhi(pr[11+e2][0]),bfhi(pr[11+e2][2])}, w2={bflo(pr[11+e2][1]),bflo(pr[11+e2][3])}, w3={bfhi(pr[11+e2][1]),bfhi(pr[11+e2][3])};
        f32x2 kf[7];
        #pragma unroll
        for(int i=0;i<7;i++) kf[i]=f32x2{bflo(pr[i][e2]),bfhi(pr[i][e2])};
        f32x2 o[4];
        #pragma unroll
        for(int i=0;i<4;i++){ f32x2 ks=kf[i]*w3; ks=kf[i+1]*w2+ks; ks=kf[i+2]*w1+ks; ks=kf[i+3]*w0+ks; o[i]=ks*f32x2{bflo(pr[7+i][e2]),bfhi(pr[7+i][e2])}; }
        #pragma unroll
        for(int z=0;z<2;z++){ u32x2 pk={cvtpk(o[3][z],o[2][z]),cvtpk(o[1][z],o[0][z])}; lw64(lw_+(2*e2+z)*128,pk); }
      }
      STAMP3(10);
      BARRIER();
      STAMP3(11);
      u32x4 v[4];
      #pragma unroll
      for(int i=0;i<4;i++) v[i]=lr128(P+((tid>>3)+32*i)*128+(((tid&7)^((w>>1)+2*i))*16));
      #pragma unroll
      for(int i=0;i<4;i++){ const u32x4 o= (w&1)? u32x4{v[i][2],v[i][3],v[i][0],v[i][1]} : v[i]; st4s<0>(o,vu,rws,i*131072); }
    } else {
      u32x4 pr[12];
      const u32x4 rwd=mk_rsrc(wd,F_*D_*2);
      const unsigned vo=(lane>>3)*1280+(lane&7)*16; unsigned so[3], sd[3]; int c4[3];
      #pragma unroll
      for(int z=0;z<3;z++){ const int un=4*g1id+w+256*z, s=un/10, c64=un-10*s; so[z]=rfl(un<540? (32*s*D_+c64*64)*2 : 0x10000000); sd[z]=rfl(un<540? WS_WDF+s*5120 : 0x10000000); c4[z]=4*c64; }
      SFOR(12,i) ld4n<0>(pr[i],vo,rwd,so[i>>2]+(i&3)*8*1280); SEND
      unsigned tk; middle(IC(12),tk);
      waitv<13>(pr[0],pr[1],pr[2],pr[3],pr[4],pr[5],pr[6],pr[7],pr[8],pr[9],pr[10],pr[11],tk);
      pinpf();
      const unsigned L=P+w*12288;
      #pragma unroll
      for(int z=0;z<3;z++){
        #pragma unroll
        for(int i=0;i<4;i++){ piece(4*z+i,1,1); const int r=8*i+(lane>>3); lw128(L+z*4096+r*128+(((lane&7)^((r>>2)&7))*16),pr[4*z+i]); } }
      BARRIER();
      const int qq=(lane&15)>>2, p=lane&3, r=4*gq+qq;
      SFOR(12,i) constexpr int z=i>>2, f4=i&3; const int ch=2*f4+(p>>1);
        const u32x4 v=cat(trread(L+z*4096+r*128+((ch^((r>>2)&7))*16)+(p&1)*8),trread(L+z*4096+(r+16)*128+((ch^(((r+16)>>2)&7))*16)+(p&1)*8));
        const int D16=c4[z]+f4, n5=D16/5, dt=D16-5*n5; unsigned so2=rfl(sd[z]+n5*276480+dt*1024); asm volatile("s_nop 4":"+s"(so2)); st4s<0>(v,vw0,rws,so2); SEND
    }
    STAMPM(2);
    const int wm=w>>1, wn=w&1;
    unsigned xa[2], wa[4];
    { const int qq=(lane&15)>>2, p=lane&3;
      #pragma unroll
      for(int j=0;j<2;j++) xa[j]=opq(lds0+(64*wm+gj)*128+(((4*j+gq)^((lane>>1)&7))*16));
      #pragma unroll
      for(int z=0;z<4;z++) wa[z]=opq(lds0+16384+(8*gq+qq)*256+(((4*wn+z)^(qq|((gq&1)<<2)))*32)+p*8); }
    f32x4 ag[4][4], au[4][4];
    u32x4 fw[4], fx[2][4];
    u32x4 fp0,fp1; u32x2 fq, st[4]; unsigned csp[2];
    auto rdw=[&](auto GC,auto JC,auto ZC,auto HC) __attribute__((always_inline)) {
      constexpr int G=decltype(GC)::value, J=decltype(JC)::value, Z=decltype(ZC)::value, Hh=decltype(HC)::value;
      const u32x2 t=__builtin_bit_cast(u32x2,trread(wa[Z&3]+J*8192+(Z>>2)*16384+Hh*1024));
      if constexpr(Hh==0){ u32x4 v={t[0],t[1],t[0],t[1]}; fw[G&3]=v; } else { fw[G&3][2]=t[0]; fw[G&3][3]=t[1]; } };
        WAITV(16); BARRIER();
    #pragma unroll
    for(int m=0;m<4;m++) fx[0][m]=lr128(xa[0]+m*2048);
    SFOR(3,z) rdw(IC(z),IC(0),IC(z),IC(0)); rdw(IC(z),IC(0),IC(z),IC(1)); SEND
    STAMPM(3); STAMP5(1);
    SFOR(10,s)
      SFOR(2,j)
        constexpr int c=j&1,n=c^1;
        SFOR(8,z)
          constexpr int G=(2*s+j)*8+z;
          SFOR(4,m)
            f32x4& A= z<4? ag[z&3][m] : au[z&3][m];
            if constexpr(s==0&&j==0) MFMA16Z(A,fw[G&3],fx[c][m]); else MFMA16(A,fw[G&3],fx[c][m]);
            if constexpr(j==0){
              if constexpr(m<2){ if constexpr(z<5) rdw(IC(G+3),IC(0),IC(z+3),IC(m)); else rdw(IC(G+3),IC(1),IC(z-5),IC(m)); }
              if constexpr(m==2 && z>=4) fx[n][z-4]=lr128(xa[1]+(z-4)*2048);
            } else {
              if constexpr(z<5 && m<2) rdw(IC(G+3),IC(1),IC(z+3),IC(m));
              if constexpr(s<9 && z==6){ if constexpr(m<2){ rdw(IC(G+2),IC(0),IC(0),IC(m)); fx[n][m]=lr128(xa[0]+m*2048); } else rdw(IC(G+3),IC(0),IC(1),IC(m-2)); }
              if constexpr(s<9 && z==7 && m<2){ rdw(IC(G+3),IC(0),IC(2),IC(m)); fx[n][2+m]=lr128(xa[0]+(2+m)*2048); }
            }
            if constexpr(s==0&&j==0){ if constexpr(z<6 && (m&1)) piece(2*z+(m>>1),2,2); }
            else if constexpr(m==3){
              if constexpr(j==1 && z>=6 && s+3<10) piece(z-6,s+3,(s+3)%3);
              if constexpr(j==0 && s+2<10) piece(2+z,s+2,(s+2)%3);
              if constexpr(j==1 && z<2 && s>=1 && s+2<10) piece(10+z,s+2,(s+2)%3);
              if constexpr(s==7 && j==1 && z==2){ const unsigned vo=WS_FP+lane*32; LD4S(fp0,vo,rws,0,0); LD4S(fp1,vo,rws,0,16);
                const unsigned vq=(WS_FQ+lane*8)|(lane<32?0:OOB); LD2S(fq,vq,rws,0,0); }
              if constexpr(s==8 && j==0 && (z==1||z==5)) conv_issue(ldsC,chl,z>>2);
            }
            GAP();
          SEND
          if constexpr(j==1 && z==5 && s<9){
            if constexpr(s==0) WAITV(16); else if constexpr(s<=6) WAITV(12); else if constexpr(s==7) WAITV(15); else WAITV(8);
            if constexpr(s==8){
              pin(fp0,fp1,fq);
              if(!__all(tag4(fp0,tag)&&tag4(fp1,tag)&&((fq[0]==(unsigned)tag&&fq[1]==(unsigned)(tag>>32))||lane>=32))){
                spin4(rws,WS_FP,64,tag); spin4(rws,WS_FQ,8,tag); }
              if(b==0 && tid==0) put_flag(ws,WS_E,tag);
              #pragma unroll
              for(int m=0;m<4;m++){ const unsigned vo=WS_ST+(128*mi+64*wm+16*m+gj)*8; LD2S(st[m],vo,rws,0,0); }
              const int col= tid<128? ni*128+tid : 1728+ni*128+tid-128; const unsigned vo=(WS_CS+col*4)|(((tid&127)+ni*128<1728)?0:OOB);
              LD1S(csp[0],vo,rws,0,0); { const unsigned so=13824; LD1S(csp[1],vo,rws,so,0); }
              conv_issue(ldsC,chl,2);
            }
            BARRIER();
            STAMP5(s+2);
            if constexpr(s==1) if(tid==0) put_flag(ws,WS_FP+b*8,tag);
            constexpr int dl= (s+1)%3==0? -2*STG : STG;
            xa[0]=opq(xa[0]+dl); xa[1]=opq(xa[1]+dl); wa[0]=opq(wa[0]+dl); wa[1]=opq(wa[1]+dl); wa[2]=opq(wa[2]+dl); wa[3]=opq(wa[3]+dl);
            GAP();
          }
        SEND
      SEND
    SEND
    STAMPM(4); STAMP5(11);
    waitv<2>(csp[0],csp[1],st[0],st[1],st[2],st[3]);
    *(LAS float*)(size_t)(lds0+L_TAB+tid*4)=__uint_as_float(csp[0])+__uint_as_float(csp[1]);
    BARRIER();
    NOP8();
    { const float L2E=1.4426950408889634f;
      f32x4 cg[4], cu[4];
      #pragma unroll
      for(int z=0;z<4;z++){ cg[z]=lr128f(lds0+L_TAB+(64*wn+16*z+4*gq)*4); cu[z]=lr128f(lds0+L_TAB+512+(64*wn+16*z+4*gq)*4); }
      SFOR(4,i) ldwd(IC(i)); SEND
      SFOR(4,m)
        const float mean=__uint_as_float(st[m][0]), r=__uint_as_float(st[m][1]);
        const float R=-L2E*r, C=-r*(1.f/L2E), nm=-mean;
        SFOR(2,G)
          u32x4 o;
          SFOR(4,te)
            constexpr int T=te>>1, e=te&1, z=2*G+T;
            f32x2 g={ag[z][m][2*e],ag[z][m][2*e+1]}, u={au[z][m][2*e],au[z][m][2*e+1]};
            g=f32x2{cg[z][2*e],cg[z][2*e+1]}*nm+g; u=f32x2{cu[z][2*e],cu[z][2*e+1]}*nm+u;
            f32x2 s=g*R; f32x2 ex={__builtin_amdgcn_exp2f(s[0]),__builtin_amdgcn_exp2f(s[1])};
            f32x2 d=ex+1.f; f32x2 rc={__builtin_amdgcn_rcpf(d[0]),__builtin_amdgcn_rcpf(d[1])};
            f32x2 h=((u*C)*s)*rc; o[2*T+e]=cvtpk(h[0],h[1]);
            ldwd(IC(4+(m*2+G)*5+te));
            GAP();
          SEND
          const int s32=ni*4+2*wn+G, m16=mi*8+wm*4+m;
          const unsigned vo=(WS_HF+((m16*54+s32)*64+lane)*16)|(s32<54?0:OOB); ST4S(o,vo,rws,0,0);
          ldwd(IC(4+(m*2+G)*5+4));
        SEND
      SEND }
    STAMPM(5);
    WAITV(52);
  } else {
    unsigned tk0;
    {
      const int gw=4*spid+w, cbk=gw>>1, kh=gw&1; const bool act=gw<108;
      const u32x4 rcs=mk_rsrc(cbk<27? wg:wu, D_*F_*2); const int cb64=cbk<27? cbk: cbk-27;
      unsigned vc[2];
      #pragma unroll
      for(int i=0;i<2;i++){ const int r=8*i+(lane>>3), cp=lane&7; const int gs=(cp>>1)^(((r>>1)&1)|((i&1)<<1)); vc[i]=((320*kh+r)*3456+cb64*128+gs*32+(cp&1)*16)|(act?0:OOB); }
      const unsigned LB=rfl(lds0+w*16384);
      auto csissue=[&](int c) __attribute__((always_inline)) {
        #pragma unroll
        for(int i=0;i<8;i++){ SETM0(LB+(c&1)*8192+i*1024); const unsigned so=(64*c+16*(i>>1))*3456; dmap<NTCS,0>(vc[i&1],rcs,so); } };
      { const unsigned vo=((w<2? 4*spid+2*w : 128+2*b)*4096+lane*128)|((w<3&&TKM)?0:OOB); if constexpr(TKM==2) ld1n<0>(tk0,vo,rhk,0); else LD1(tk0,vo,rhk,0,0); }
      csissue(0); csissue(1);
      waitv<17+NPF>(Eraw,xc3[0],xc3[1],xc3[2]);
      stats();
      STAMPM(1);
      WAITV(0); pinpf();
      BARRIER();
      if(tid==0) put_flag(ws,WS_FP+b*8,tag);
      const int qq=(lane&15)>>2, p=lane&3; f32x4 cs[4]; u32x4 on={0x3f803f80u,0x3f803f80u,0x3f803f80u,0x3f803f80u}; asm volatile("s_nop 1":"+v"(on));
      SFOR(5,c)
        if constexpr(c>=1 && c<4) WAITV(8); else if constexpr(c==4) WAITV(0);
        u32x4 fa[2][4];
        SFOR(2,k) SFOR(4,z) const int r0=32*k+8*gq+qq; const int g0=((r0>>1)&1)|(((r0>>3)&1)<<1);
          fa[k][z]=cat(trread(LB+(c&1)*8192+r0*128+((z^g0)*32)+p*8),trread(LB+(c&1)*8192+(r0+4)*128+((z^g0)*32)+p*8)); SEND SEND
        WAITL();
        if constexpr(c+2<5) csissue(c+2);
        GAP();
        SFOR(2,k) SFOR(4,z) if constexpr(c==0&&k==0) MFMA16Z(cs[z],fa[k][z],on); else MFMA16(cs[z],fa[k][z],on); SEND SEND
        GAP();
      SEND
      NOP8(); pin(cs[0],cs[1],cs[2],cs[3]);
      f32x4 v;
      #pragma unroll
      for(int e=0;e<4;e++) v[e]= gj==0? cs[0][e] : gj==1? cs[1][e] : gj==2? cs[2][e] : cs[3][e];
      const unsigned vo=(WS_CS+(kh*3456+cbk*64+16*gj+4*gq)*4)|((act&&gj<4)?0:OOB); ST4S(__builtin_bit_cast(u32x4,v),vo,rws,0,0);
    }
    STAMPM(3);
    BARRIER();
    conv_issue(ldsR0,4*spid+cvp,0); conv_issue(ldsR0,4*spid+cvp,1);
    conv_issue(ldsR1,4*spid+2+cvp,0); conv_issue(ldsR1,4*spid+2+cvp,1);
    conv_issue(ldsC,chl,0); conv_issue(ldsC,chl,1);
    waitv<24>(tk0); BARRIER();
    if(tid==0) put_flag(ws,WS_FQ+spid*8,tag);
    STAMPM(4);
    STAMPM(5);
  }
  u32x2 ph, xv[5], stv;
  const int trow=64*mt+16*w+gj;
  u32x4 H[14][4];
  unsigned vh[4];
  #pragma unroll
  for(int r=0;r<4;r++) vh[r]=WS_HF+((w+r)&3)*54*1024+lane*16;
  auto hoff=[&](int i) __attribute__((always_inline)) ->unsigned { return i<13? hbase+i*1024 : h13; };
  auto ldH=[&](int i) __attribute__((always_inline)) { const unsigned sh=hoff(i);
    LD4S(H[i][0],vh[0],rws,sh,0); LD4S(H[i][1],vh[1],rws,sh,0); LD4S(H[i][2],vh[2],rws,sh,0); LD4S(H[i][3],vh[3],rws,sh,0); };
  auto conv_round=[&](auto LASTC, const unsigned base, const int ch) __attribute__((always_inline)) {
    constexpr bool LAST=decltype(LASTC)::value;
    BARRIER();
    if(cvh==0){
      const int c=lane>>4,p=lane&15; const LAS u16* src=(const LAS u16*)(size_t)(base+32+ (p>=16-c? (p-16+c)*2:0));
      u16 v=*src; *(LAS u16*)(size_t)(base+c*CV_HC+p*2)= p>=16-c? v:(u16)0;
    } else if(lane<32) lw128(base+CV_H+4096+lane*16,u32x4{0,0,0,0});
    const float Dd=bflo(((const unsigned*)Dv)[ch>>1]>>((ch&1)*16));
    BARRIER();
    if constexpr(LAST){ STAMPL(6); }
    const int ci=gj,cq=gq,cc=(ci+1)&3;
    const unsigned pa=opq(base+cc*CV_HC+2*(1+ci-cc+8*cq)+256*cvh), pb=opq(base+CV_H+2*(2032-16*ci+8*cq)-3584+256*cvh);
    f32x4 ya[8];
    { u32x4 B[2][8], A[2][8];
      auto ldA=[&](int rho,int a)->u32x4{ const unsigned p=pa+64*rho+512*a; return cat2(lr64v(p),lr64v(p+8)); };
      auto ldB=[&](int rho,int bb)->u32x4{ return lr128(pb+64*rho+512*(7-bb)); };
      #pragma unroll
      for(int z=0;z<8;z++){ B[0][z]=ldB(0,z); A[0][z]=ldA(0,z); }
      GAP();
      SFOR(4,rho)
        constexpr int c=rho&1,n=c^1;
        if constexpr(LAST && rho==2){
          WAITV(15); if(isg && lane==0) put_flag(ws,WS_FH+(mi*64+ni*4+w)*8,tag); }
        if constexpr(LAST && rho==3){ const unsigned vo=(WS_FH+((mt>>1)*64+lane)*8)|(lane<56?0:OOB); LD2S(ph,vo,rws,0,0); }
        SFOR(8,a)
          SFOR(8-a,nn_)
            constexpr int nu=a+nn_; constexpr int cnt=a*8-(a*(a-1))/2+(nu-a);
            if constexpr(rho==0 && a==0) MFMA16Z(ya[nu],A[c][a],B[c][nu-a]); else MFMA16(ya[nu],A[c][a],B[c][nu-a]);
            if constexpr(rho<3){
              if constexpr(cnt<8) B[n][cnt]=ldB(rho+1,cnt);
              else if constexpr(cnt<24){ constexpr int az=(cnt-8)>>1; const u32x2 t=lr64v(pa+64*(rho+1)+512*az+8*(cnt&1));
                if constexpr((cnt&1)==0){ u32x4 v={t[0],t[1],t[0],t[1]}; A[n][az]=v; } else { A[n][az][2]=t[0]; A[n][az][3]=t[1]; } } }
            if constexpr(LAST && rho<3 && cnt>=24 && cnt<31){ constexpr int nn=NWE+rho*7+(cnt-24); if constexpr(nn<64) ldwd(IC(nn)); }
            if constexpr(LAST && rho==3 && cnt>=4 && cnt<28 && cnt%4==0){
              constexpr int q=cnt/4-1; const unsigned vo=(trow*D_+80*nt+4*gq)*2;
              if constexpr(q<5) ld2<32*q>(xv[q],vo,rx,0); else { const unsigned vs=WS_ST+trow*8; LD2S(stv,vs,rws,0,0); } }
            GAP();
          SEND
        SEND
      SEND
    }
    if constexpr(LAST){ STAMPL(7);
      waitv<6>(ph);
      if(!__all((ph[0]==(unsigned)tag && ph[1]==(unsigned)(tag>>32)) || lane>=56)) spin4(rws,WS_FH+(mt>>1)*512,14,tag);
      ldH(0); ldH(1); ldH(2); ldH(3); }
    NOP8();
    f32x4 mine[4];
    { const unsigned R=lds0+L_RED+w*4096+lane*16;
      #pragma unroll
      for(int z=0;z<4;z++){ const f32x4 lo=ya[z], hi=ya[4+z]; f32x4 snd, kp;
        #pragma unroll
        for(int e=0;e<4;e++){ snd[e]= cvh? lo[e]:hi[e]; kp[e]= cvh? hi[e]:lo[e]; }
        lw128f(R+z*1024,snd); mine[z]=kp; } }
    BARRIER();
    { const unsigned R=lds0+L_RED+(w^1)*4096+lane*16; unsigned pk[4][2];
      #pragma unroll
      for(int z=0;z<4;z++){ const f32x4 y=mine[z]+lr128f(R+z*1024);
        const int t=256*(4*cvh+z)+16*ci+4*cq;
        const u32x2 uu=*(const LAS u32x2*)(size_t)(base+CV_H+2*(2044-t));
        pk[z][0]=cvtpk(fmaf(bfhi(uu[1]),Dd,y[0]),fmaf(bflo(uu[1]),Dd,y[1])); pk[z][1]=cvtpk(fmaf(bfhi(uu[0]),Dd,y[2]),fmaf(bflo(uu[0]),Dd,y[3])); }
      #pragma unroll
      for(int z=0;z<4;z+=2){
        auto r0=__builtin_amdgcn_permlane16_swap(pk[z][0],pk[z+1][0],false,false);
        auto r1=__builtin_amdgcn_permlane16_swap(pk[z][1],pk[z+1][1],false,false);
        u32x4 v={r0[0],r1[0],r0[1],r1[1]};
        const int t=256*(4*cvh+z+(cq&1))+16*ci+8*(cq>>1);
        const unsigned vo=WS_YT+ch*4096+t*2; ST4S(v,vo,rws,0,0); }
    }
  };
  if(!isg){
    spin4(rws,WS_FP,64,tag);
    conv_issue(ldsR0,4*spid+cvp,2); conv_issue(ldsR1,4*spid+2+cvp,2); conv_issue(ldsC,chl,2);
    unsigned tw[9];
    SFOR(9,i) const unsigned li=((jj-28)*4+w)*540+i*64+lane; const unsigned vt=(WS_WDF+(xcd&1)*4*276480+li*128)|((i*64+lane<540)?0:OOB); ld1s<0>(tw[i],vt,rws,0); SEND
    WAITV(13);
    STAMP4(6);
    conv_round(std::integral_constant<bool,false>{},ldsR0,4*spid+cvp);
    STAMP4(7);
    WAITV(13);
    conv_round(std::integral_constant<bool,false>{},ldsR1,4*spid+2+cvp);
    STAMP4(8);
    waitv<2>(tw[0],tw[1],tw[2],tw[3],tw[4],tw[5],tw[6],tw[7],tw[8]);
    if(lane==0) put_flag(ws,WS_FY+((4*spid+cvp)*2+cvh)*8,tag);
    spin4(rws,WS_FQ,8,tag);
    if(lane==0) put_flag(ws,WS_FY+((4*spid+2+cvp)*2+cvh)*8,tag);
    STAMP4(9);
    SFOR(NWE,n) ldwd(IC(n)); SEND
    STAMP4(10);
  }
  conv_round(std::integral_constant<bool,true>{},ldsC,chl);
  STAMPL(8);
  u32x4 WV[6]; u32x4 py0,py1; u32x4 yv[3];
  f32x4 oa[4][5];
  #define HREGS(i) H[i][0],H[i][1],H[i][2],H[i][3]
  SFOR(14,i)
    if constexpr(i==0){ waitv<14>(HREGS(0),xv[0],xv[1],xv[2],xv[3],xv[4],stv); }
    if constexpr(i==1){ waitv<14>(HREGS(1)); STAMP2(1);
                        }
    if constexpr(i==2) waitv<14>(HREGS(2));
    if constexpr(i==3){ waitv<12>(HREGS(3)); STAMP2(2);
                        if(lane==0) put_flag(ws,WS_FY+(chl*2+cvh)*8,tag); }
    if constexpr(i==4) waitv<12>(HREGS(4));
    if constexpr(i==5) waitv<18>(HREGS(5));
    if constexpr(i==6) waitv<20>(HREGS(6));
    if constexpr(i==7){ waitv<20>(HREGS(7)); STAMP2(3); }
    if constexpr(i==8) waitv<14>(HREGS(8),WV[0],WV[1],WV[2],WV[3],WV[4],WV[5]);
    if constexpr(i==9){ waitv<12>(HREGS(9),py0,py1); STAMP2(4);
              if(!__all((tag4(py0,tag)&&tag4(py1,tag))||lane>=40)){
                spin4(rws,WS_FY+160*nt*8,40,tag); } }
    if constexpr(i==10){ waitv<12>(HREGS(10)); STAMP2(5); }
    if constexpr(i==11) waitv<11>(HREGS(11));
    if constexpr(i==12) waitv<7>(HREGS(12));
    if constexpr(i==13) waitv<3>(HREGS(13));
    GAP();
    SFOR(4,tt)
      SFOR(5,dt)
        constexpr int nn=5*i+dt, sl=tt*5+dt;
        if constexpr(i==0) mfmawz<nn>(oa[tt][dt],H[0][tt]);
        else if constexpr(nn<64) mfmaw<nn>(oa[tt][dt],H[i][tt]);
        else MFMA16(oa[tt][dt],WV[nn-64],H[i][tt]);
        if constexpr(i+4<14 && sl%4==1 && sl<16){ const unsigned sh=hoff(i+4); ld4s<0>(H[i+4][sl>>2],vh[sl>>2],rws,sh); }
        if constexpr(i==4 && sl%3==0 && sl>=2 && sl<20){ constexpr int z=sl/3-1; if constexpr(z<6){ constexpr int n2=64+z, i2=n2/5, d2=n2%5;
            const unsigned so=(i2<13? wdbase+i2*5120 : wd13)+d2*1024; ld4s<0>(WV[z],vw0,rws,so); } }
        if constexpr(i==5 && sl==17){ const unsigned vo=(WS_FY+(160*nt+4*lane)*8)|(lane<40?0:OOB); LD4S(py0,vo,rws,0,0); LD4S(py1,vo,rws,0,16); }
        if constexpr(i==10 && sl%6==2){ constexpr int z=sl/6; const int id=tid+256*z; const int r=id>>3,c8=id&7;
            const unsigned vo=(WS_YT+(80*nt+r)*4096+(64*mt+c8*8)*2)|(id<640?0:OOB); LD4S(yv[z],vo,rws,0,0); }
        GAP();
      SEND
    SEND
  SEND
  STAMPL(9);
  waitv<0>(yv[0],yv[1],yv[2]);
  #pragma unroll
  for(int z=0;z<3;z++){ const int id=tid+256*z; if(id<640) lw128(lds0+L_Y+(id>>3)*160+(id&7)*16,yv[z]); }
  BARRIER();
  NOP8();
  #pragma unroll
  for(int r=1;r<4;r++){ const unsigned a=lds0+((((w+r)&3)*3+(r-1))*5)*1024+lane*16;
    #pragma unroll
    for(int dt=0;dt<5;dt++) lw128f(a+dt*1024,oa[r][dt]); }
  BARRIER();
  STAMPL(10);
  { const float m=__uint_as_float(stv[0]), rd=__uint_as_float(stv[1]); const unsigned a=lds0+(w*15)*1024+lane*16;
    #pragma unroll
    for(int dt=0;dt<5;dt++){
      f32x4 f=(oa[0][dt]+lr128f(a+dt*1024))+(lr128f(a+(5+dt)*1024)+lr128f(a+(10+dt)*1024));
      const u32x2 y2=__builtin_bit_cast(u32x2,trread(lds0+L_Y+(16*dt+4*gq+((lane&15)>>2))*160+(16*w+4*(lane&3))*2));
      const float xf[4]={bflo(xv[dt][0]),bfhi(xv[dt][0]),bflo(xv[dt][1]),bfhi(xv[dt][1])};
      const float yf[4]={bflo(y2[0]),bfhi(y2[0]),bflo(y2[1]),bfhi(y2[1])};
      float o[4];
      #pragma unroll
      for(int e=0;e<4;e++) o[e]=fmaf((xf[e]-m)*rd,yf[e],xf[e])+f[e];
      u32x2 ov={cvtpk(o[0],o[1]),cvtpk(o[2],o[3])};
      *(u32x2*)(out+(size_t)trow*D_+80*nt+16*dt+4*gq)=ov;
    } }
  STAMPL(11);
}

extern "C" void kernel_launch(void* const* d_in, const int* in_sizes, int n_in, void* d_out, int out_size, void* d_ws, size_t ws_size, hipStream_t stream){
  (void)hipFuncSetAttribute((const void*)fused_kernel,hipFuncAttributeMaxDynamicSharedMemorySize,LDS_BYTES);
  fused_kernel<<<256,256,LDS_BYTES,stream>>>((const u16*)d_in[0],(const u16*)d_in[1],(const u16*)d_in[2],(const u16*)d_in[3],(const u16*)d_in[4],(const u16*)d_in[5],
      (const u16*)d_in[7],(const u16*)d_in[8],(const u16*)d_in[9],(u16*)d_out,(char*)d_ws);
}
```

```cpp
#include <hip/hip_runtime.h>
#include <stdint.h>
#include <type_traits>
#include <utility>
#define LAS __attribute__((address_space(3)))
#define GAS __attribute__((address_space(1)))
#define DI __device__ __forceinline__
typedef unsigned short u16;
typedef unsigned long long u64;
typedef short s16x4 __attribute__((ext_vector_type(4)));
typedef short s16x8 __attribute__((ext_vector_type(8)));
typedef __bf16 bf16x2 __attribute__((ext_vector_type(2)));
typedef float f32x2 __attribute__((ext_vector_type(2)));
typedef float f32x4 __attribute__((ext_vector_type(4)));
typedef unsigned u32x4 __attribute__((ext_vector_type(4)));
typedef unsigned u32x2 __attribute__((ext_vector_type(2)));
constexpr int S_=2048, D_=640, F_=1728;
#define GAP() __builtin_amdgcn_sched_barrier(0)
DI unsigned rfl(unsigned x){ return __builtin_amdgcn_readfirstlane(x); }
DI u32x4 mk_rsrc(const void* p, unsigned bytes){
  unsigned long long a=(unsigned long long)p;
  u32x4 r; r[0]=rfl((unsigned)a); r[1]=rfl((unsigned)(a>>32))&0xffffu; r[2]=bytes; r[3]=0x00020000u; return r;
}
DI void setm0(unsigned x){ asm volatile("s_mov_b32 m0, %0\n\ts_nop 0"::"s"(x)); }
template<int I> DI void dma (unsigned vo,const u32x4& r,unsigned so){ asm volatile("buffer_load_dwordx4 %0, %1, %2 offen offset:%3 lds"::"v"(vo),"s"(r),"s"(so),"n"(I)); }
template<int I> DI void dmas(unsigned vo,const u32x4& r,unsigned so){ asm volatile("buffer_load_dwordx4 %0, %1, %2 offen offset:%3 sc1 lds"::"v"(vo),"s"(r),"s"(so),"n"(I)); }
template<int I> DI void ld4  (u32x4& d,unsigned vo,const u32x4& r,unsigned so){ asm volatile("buffer_load_dwordx4 %0, %1, %2, %3 offen offset:%4":"=v"(d):"v"(vo),"s"(r),"s"(so),"n"(I)); }
template<int N,int I> DI void ldwa(unsigned vo,const u32x4& r,unsigned so){ asm volatile("buffer_load_dwordx4 a[%c3:%c4], %0, %1, %2 offen offset:%5 sc1"::"v"(vo),"s"(r),"s"(so),"n"(4*N),"n"(4*N+3),"n"(I)); }
template<int N> DI void mfmaw (f32x4& acc,const u32x4& b){ asm volatile("v_mfma_f32_16x16x32_bf16 %0, a[%c2:%c3], %1, %0":"+v"(acc):"v"(b),"n"(4*N),"n"(4*N+3)); }
template<int N> DI void mfmawz(f32x4& acc,const u32x4& b){ asm volatile("v_mfma_f32_16x16x32_bf16 %0, a[%c2:%c3], %1, 0":"=&v"(acc):"v"(b),"n"(4*N),"n"(4*N+3)); }
template<int I> DI void ld4s (u32x4& d,unsigned vo,const u32x4& r,unsigned so){ asm volatile("buffer_load_dwordx4 %0, %1, %2, %3 offen offset:%4 sc1":"=v"(d):"v"(vo),"s"(r),"s"(so),"n"(I)); }
template<int I> DI void ld2s (u32x2& d,unsigned vo,const u32x4& r,unsigned so){ asm volatile("buffer_load_dwordx2 %0, %1, %2, %3 offen offset:%4 sc1":"=v"(d):"v"(vo),"s"(r),"s"(so),"n"(I)); }
template<int I> DI void ld2  (u32x2& d,unsigned vo,const u32x4& r,unsigned so){ asm volatile("buffer_load_dwordx2 %0, %1, %2, %3 offen offset:%4":"=v"(d):"v"(vo),"s"(r),"s"(so),"n"(I)); }
template<int I> DI void ld1s (unsigned& d,unsigned vo,const u32x4& r,unsigned so){ asm volatile("buffer_load_dword %0, %1, %2, %3 offen offset:%4 sc1":"=v"(d):"v"(vo),"s"(r),"s"(so),"n"(I)); }
template<int I> DI void st4s (const u32x4& d,unsigned vo,const u32x4& r,unsigned so){ asm volatile("buffer_store_dwordx4 %0, %1, %2, %3 offen offset:%4 sc1\n\ts_nop 1"::"v"(d),"v"(vo),"s"(r),"s"(so),"n"(I)); }
template<int I> DI void st2s (const u32x2& d,unsigned vo,const u32x4& r,unsigned so){ asm volatile("buffer_store_dwordx2 %0, %1, %2, %3 offen offset:%4 sc1\n\ts_nop 1"::"v"(d),"v"(vo),"s"(r),"s"(so),"n"(I)); }
#define SETM0(x) setm0(x)
#define DMAS(voff,rsrc,soff,imm) dmas<imm>(voff,rsrc,soff)
#define LD4S(dst,voff,rsrc,soff,imm) ld4s<imm>(dst,voff,rsrc,soff)
#define LD2S(dst,voff,rsrc,soff,imm) ld2s<imm>(dst,voff,rsrc,soff)
#define LD1S(dst,voff,rsrc,soff,imm) ld1s<imm>(dst,voff,rsrc,soff)
#define ST4S(src,voff,rsrc,soff,imm) st4s<imm>(src,voff,rsrc,soff)
#define ST2S(src,voff,rsrc,soff,imm) st2s<imm>(src,voff,rsrc,soff)
template<int N> DI void waitv(){ asm volatile("s_waitcnt vmcnt(%0)"::"n"(N):"memory"); }
template<class T> DI void pin(T& a){ asm volatile("":"+v"(a)::"memory"); }
template<class T,class... R> DI void pin(T& a,R&... r){ pin(a); pin(r...); }
template<int N,class... R> DI void waitv(R&... r){ asm volatile("s_waitcnt vmcnt(%0)"::"n"(N):"memory"); pin(r...); }
#define WAITV(n) waitv<n>()
#define WAITL() asm volatile("s_waitcnt lgkmcnt(0)":::"memory")
#define BARRIER() asm volatile("s_waitcnt lgkmcnt(0)\n\ts_barrier":::"memory")
DI void MFMA16  (f32x4& acc,const u32x4& a,const u32x4& b){ asm volatile("v_mfma_f32_16x16x32_bf16 %0, %1, %2, %0":"+v"(acc):"v"(a),"v"(b)); }
DI void MFMA16Z (f32x4& acc,const u32x4& a,const u32x4& b){ asm volatile("v_mfma_f32_16x16x32_bf16 %0, %1, %2, 0":"=&v"(acc):"v"(a),"v"(b)); }
DI void NOP8(){ asm volatile("s_nop 7":::"memory"); __builtin_amdgcn_sched_barrier(0); }
DI float bflo(unsigned x){ return __uint_as_float(x<<16); }
DI float bfhi(unsigned x){ return __uint_as_float(x&0xffff0000u); }
DI unsigned cvtpk(float lo,float hi){ f32x2 v={lo,hi}; bf16x2 b=__builtin_convertvector(v,bf16x2); return __builtin_bit_cast(unsigned,b); }
DI float rbf(float x){ return bflo(cvtpk(x,x)); }
DI u32x4 cat(s16x4 a, s16x4 b){ s16x8 r=__builtin_shufflevector(a,b,0,1,2,3,4,5,6,7); return __builtin_bit_cast(u32x4,r); }
DI u32x4 cat2(u32x2 a,u32x2 b){ return __builtin_shufflevector(a,b,0,1,2,3); }
DI s16x4 trread(unsigned a){ return __builtin_amdgcn_ds_read_tr16_b64_v4i16((LAS s16x4*)(size_t)a); }
DI u32x4 lr128(unsigned a){ return *(const LAS u32x4*)(size_t)a; }
DI f32x4 lr128f(unsigned a){ return *(const LAS f32x4*)(size_t)a; }
DI u32x2 lr64v(unsigned a){ return *(const volatile LAS u32x2*)(size_t)a; }
DI void lw128(unsigned a,u32x4 v){ *(LAS u32x4*)(size_t)a=v; }
DI void lw128f(unsigned a,f32x4 v){ *(LAS f32x4*)(size_t)a=v; }
DI void lw64(unsigned a,u32x2 v){ *(LAS u32x2*)(size_t)a=v; }
DI unsigned opq(unsigned x){ asm volatile("":"+v"(x)); return x; }
template<int CTRL> DI float dpp_add(float v){ return v+__builtin_bit_cast(float,__builtin_amdgcn_update_dpp(0,__builtin_bit_cast(int,v),CTRL,0xf,0xf,true)); }
DI float sum32(float v){
  v=dpp_add<0x128>(v); v=dpp_add<0x124>(v); v=dpp_add<0x122>(v); v=dpp_add<0x121>(v);
  auto r=__builtin_amdgcn_permlane16_swap(__float_as_uint(v),__float_as_uint(v),false,false);
  return __uint_as_float(r[0])+__uint_as_float(r[1]); }

constexpr unsigned OOB=0xfff00000u;
template<int... I,class F> DI void sfor_impl(std::integer_sequence<int,I...>,F&& f){ (f(std::integral_constant<int,I>{}),...); }
template<int N,class F> DI void sfor(F&& f){ sfor_impl(std::make_integer_sequence<int,N>{},f); }
#define SFOR(N,v) sfor<N>([&](auto v##_) __attribute__((always_inline)) { constexpr int v=v##_;
#define SEND });
#define IC(v) std::integral_constant<int,(v)>{}
constexpr unsigned WS_E=0, WS_FP=256, WS_FQ=WS_FP+2048, WS_FH=WS_FQ+256, WS_FY=WS_FH+8192, WS_FLAGS_END=WS_FY+10240;
constexpr unsigned WS_ST=24576  , WS_CS=WS_ST+16384  , WS_UT=WS_CS+57344, WS_WDF=WS_UT+D_*S_*2, WS_HF=WS_WDF+F_*D_*2, WS_YT=WS_HF+S_*F_*2, WS_END=WS_YT+D_*S_*2;
static_assert(WS_FLAGS_END<=WS_ST,"");
constexpr int STG=49152;
constexpr int L_KV=2*STG;
constexpr int L_TAB=3*STG;
constexpr int CV_HC=4160, CV_H=4*CV_HC, CV_U=4608, CV_CH=CV_H+CV_U;
constexpr int L_CV=STG;
constexpr int L_RED=2*STG;
constexpr int L_Y=L_TAB;
constexpr int LDS_BYTES=163840;

DI void spin4(const u32x4& rws, unsigned off, int nlanes, u64 tag){
  const int lane=threadIdx.x&63; const unsigned vo=(off+lane*32)|(lane<nlanes?0:OOB); const unsigned tl=(unsigned)tag, th=(unsigned)(tag>>32);
  const u64 t0=__builtin_amdgcn_s_memrealtime();
  for(;;){
    u32x4 a,c; ld4s<0>(a,vo,rws,0); ld4s<16>(c,vo,rws,0); waitv<0>(a,c);
    const bool ok= a[0]==tl&&a[1]==th&&a[2]==tl&&a[3]==th&&c[0]==tl&&c[1]==th&&c[2]==tl&&c[3]==th;
    if(__all(ok || lane>=nlanes)) break;
    if(__builtin_amdgcn_s_memrealtime()-t0>2000000ull) break;
    __builtin_amdgcn_s_sleep(1);
  }
}
DI void put_flag(char* ws, unsigned off, u64 tag){ __hip_atomic_store((GAS u64*)(ws+off),tag,__ATOMIC_RELAXED,__HIP_MEMORY_SCOPE_AGENT); }
DI bool tag4(u32x4 v,u64 tag){ const unsigned tl=(unsigned)tag, th=(unsigned)(tag>>32); return v[0]==tl&&v[1]==th&&v[2]==tl&&v[3]==th; }

__global__ __launch_bounds__(256,1) void fused_kernel(const u16* __restrict__ x,const u16* __restrict__ kk,const u16* __restrict__ vv,const u16* __restrict__ sk,
    const u16* __restrict__ hk,const u16* __restrict__ Dv,const u16* __restrict__ wg,const u16* __restrict__ wu,const u16* __restrict__ wd,u16* __restrict__ out,char* ws){
  extern __shared__ __attribute__((aligned(16))) char smem[];
  asm volatile("":::"a0","a255");
  asm volatile(""::"s"(x),"s"(kk),"s"(vv),"s"(sk),"s"(hk),"s"(Dv),"s"(wg),"s"(wu),"s"(wd),"s"(out),"s"(ws));
  const int tid=threadIdx.x, lane=tid&63; const unsigned w=rfl(tid>>6); const int b=blockIdx.x;
  const unsigned lds0=(unsigned)(size_t)(LAS char*)smem;
  const u32x4 rws=mk_rsrc(ws,WS_END), rx=mk_rsrc(x,S_*D_*2);
  u32x2 Eraw; { const unsigned z=0; LD2S(Eraw,z,rws,0,0); }
  const int lrow=8*b+2*w+(lane>>5), l32=lane&31;
  auto ldx3=[&](u32x4 (&xc3)[3]) __attribute__((always_inline)) { const unsigned vo=lrow*1280+l32*16; ld4<0>(xc3[0],vo,rx,0); ld4<512>(xc3[1],vo,rx,0); const unsigned v2=(vo+1024)|(l32<16?0:OOB); ld4<0>(xc3[2],v2,rx,0); };
  const int xcd=b&7, jj=b>>3; const bool isg=jj<28;
  const int g1id=xcd*28+jj, spid=xcd*4+jj-28;
  const int mi=(xcd>>1)*4+(jj&3), ni=(xcd&1)*7+(jj>>2);
  const int gj=lane&15,gq=lane>>4;
  const bool iskv=isg && g1id>=64; const int kvt=g1id-64;
  const int tb=kvt/5, cb=kvt-5*tb, t0=tb*64, pc_=tid&15, tg=tid>>4, d0=cb*128+pc_*8;
  const u32x4 rg=mk_rsrc(wg,D_*F_*2), ru=mk_rsrc(wu,D_*F_*2);
  unsigned vx[4], vw[4];
  #pragma unroll
  for(int i=0;i<4;i++){ const int row=32*w+8*i+(lane>>3); const int c=(lane&7)^((row>>1)&7); vx[i]=(128*mi+row)*1280+c*16-1024*i;
    const int k=16*w+4*i+(lane>>4), cp=lane&15, gs=(cp>>1)^((k&3)|(((k>>3)&1)<<2)); vw[i]=(k*3456+ni*256+gs*32+(cp&1)*16-1024*i)|((ni*128+gs*16>=F_)?OOB:0); }
  const unsigned m0x=rfl(lds0+w*4096), m0g=m0x+16384, m0u=m0x+32768;
  auto piece=[&](int P,int st,int bf) __attribute__((always_inline)) {
    if(P==0) SETM0(m0x+bf*STG); if(P==4) SETM0(m0g+bf*STG); if(P==8) SETM0(m0u+bf*STG);
    const unsigned sx=st*128, sw=st*221184;
    if(P==0) dma<0>(vx[0],rx,sx); if(P==1) dma<1024>(vx[1],rx,sx); if(P==2) dma<2048>(vx[2],rx,sx); if(P==3) dma<3072>(vx[3],rx,sx);
    if(P==4) dma<0>(vw[0],rg,sw); if(P==5) dma<1024>(vw[1],rg,sw); if(P==6) dma<2048>(vw[2],rg,sw); if(P==7) dma<3072>(vw[3],rg,sw);
    if(P==8) dma<0>(vw[0],ru,sw); if(P==9) dma<1024>(vw[1],ru,sw); if(P==10) dma<2048>(vw[2],ru,sw); if(P==11) dma<3072>(vw[3],ru,sw);
  };
  u64 tag;
  auto stats=[&](u32x4 (&xc3)[3]) __attribute__((always_inline)) {
  tag=((u64)rfl(Eraw[0])|((u64)rfl(Eraw[1])<<32))*6364136223846793005ull+1442695040888963407ull;
  {
    f32x2 f[12];
    #pragma unroll
    for(int i=0;i<3;i++) for(int e=0;e<4;e++) f[4*i+e]=f32x2{bflo(xc3[i][e]),bfhi(xc3[i][e])};
    f32x2 s0=(f[0]+f[1])+(f[2]+f[3]), s1=(f[4]+f[5])+(f[6]+f[7]), s2=(f[8]+f[9])+(f[10]+f[11]); s0=(s0+s1)+s2;
    const float mean=sum32(s0[0]+s0[1])*(1.f/640.f);
    f32x2 q0={0.f,0.f},q1={0.f,0.f},q2={0.f,0.f};
    #pragma unroll
    for(int i=0;i<4;i++){ f32x2 d=f[i]-mean, e=f[4+i]-mean, g=f[8+i]-mean; q0=d*d+q0; q1=e*e+q1; q2=g*g+q2; }
    q0+=q1;
    const float var=rbf(sum32(q0[0]+q0[1]+(l32<16? q2[0]+q2[1]:0.f))*(1.f/640.f)); const float m=rbf(mean);
    const float den=rbf(__builtin_amdgcn_sqrtf(rbf(var+1e-5f)));
    u32x2 sv={__float_as_uint(m),__float_as_uint(__builtin_amdgcn_rcpf(den))}; const unsigned vo=(WS_ST+lrow*8)|(l32==0?0:OOB);
    ST2S(sv,vo,rws,0,0);
  }
  };
  const int mt=(xcd>>1)*8+(jj&7), nt=(xcd&1)*4+(jj>>3);
  const u32x4 rhk=mk_rsrc(hk,D_*S_*2);
  const int cvp=w>>1, cvh=w&1;
  const unsigned ldsC=rfl(lds0+L_CV+cvp*CV_CH);
  const int chl=128+2*b+cvp;
  const unsigned vw0=lane*16, vw1=vw0+4096;
  const int kst=14*w-(w==3?1:0);
  const unsigned wdbase=rfl(WS_WDF+(nt*54+kst)*5120), hbase=rfl((4*mt*54+kst)*1024);
  const unsigned wd13=rfl(w<2? wdbase+13*5120 : 0x10000000u), h13=rfl(w<2? hbase+13*1024 : 0x10000000u);
  auto ldwd=[&](auto NC) __attribute__((always_inline)) { constexpr int n=decltype(NC)::value, i=n/5, dt=n%5; static_assert(i<13,"");
    const unsigned so=wdbase+i*5120; if constexpr(dt<4) ldwa<n,dt*1024>(vw0,rws,so); else ldwa<n,0>(vw1,rws,so); };
  auto conv_piece=[&](unsigned base,unsigned so,int k) __attribute__((always_inline)) {
    if(k<8){ const int c=2*cvh+(k>>2); const unsigned vo=lane*16+2*c; if((k&3)==0) SETM0(base+c*CV_HC+32);
      if((k&3)==0) dma<0>(vo,rhk,so); if((k&3)==1) dma<1024>(vo,rhk,so); if((k&3)==2) dma<2048>(vo,rhk,so); if((k&3)==3) dma<3072>(vo,rhk,so); }
    else { const unsigned vo=WS_UT+lane*16+cvh*2048; if(k==8){ SETM0(base+CV_H+cvh*2048); DMAS(vo,rws,so,0); } else DMAS(vo,rws,so,1024); }
  };
  auto chso=[&](int ch) __attribute__((always_inline)) ->unsigned { unsigned so=rfl(ch*4096); asm volatile("s_nop 4":"+s"(so)); return so; };
  auto conv_issue=[&](unsigned base,int ch,int part) __attribute__((always_inline)) {
    const unsigned so=chso(ch);
    if(part<2){ conv_piece(base,so,4*part); conv_piece(base,so,4*part+1); conv_piece(base,so,4*part+2); conv_piece(base,so,4*part+3); }
    else { conv_piece(base,so,8); conv_piece(base,so,9); }
  };
  const unsigned soC=chso(chl);
  float DdC=bflo(((const unsigned*)Dv)[chl>>1]>>((chl&1)*16)); asm volatile("":"+v"(DdC));
  const unsigned ldsR0=rfl(lds0+cvp*CV_CH), ldsR1=rfl(lds0+2*STG+16384+cvp*CV_CH);
  constexpr int NWE=44;
  if(isg){
    #pragma unroll
    for(int P=0;P<12;P++) piece(P,0,0);
    auto middle=[&](auto NLC, u32x4 (&xc3)[3]) __attribute__((always_inline)) { constexpr int NL=decltype(NLC)::value;
      waitv<NL>(Eraw,xc3[0],xc3[1],xc3[2]);
      stats(xc3);
 };
    const unsigned P=lds0+L_KV;
    if(iskv){
      u32x4 xc3[3]; ldx3(xc3);
      u32x4 pr[15];
      const u32x4 rk=mk_rsrc(kk,S_*D_*2), rv=mk_rsrc(vv,S_*D_*2), rsk=mk_rsrc(sk,D_*8);
      const unsigned vb=((t0+4*tg)*D_+d0)*2, va=(vb-3840)|((t0+tg==0)?OOB:0), vs=d0*8;
      ld4<0>(pr[0],va,rk,0); ld4<1280>(pr[1],va,rk,0); ld4<2560>(pr[2],va,rk,0);
      ld4<0>(pr[3],vb,rk,0); ld4<1280>(pr[4],vb,rk,0); ld4<2560>(pr[5],vb,rk,0); ld4<3840>(pr[6],vb,rk,0);
      ld4<0>(pr[7],vb,rv,0); ld4<1280>(pr[8],vb,rv,0); ld4<2560>(pr[9],vb,rv,0); ld4<3840>(pr[10],vb,rv,0);
      ld4<0>(pr[11],vs,rsk,0); ld4<16>(pr[12],vs,rsk,0); ld4<32>(pr[13],vs,rsk,0); ld4<48>(pr[14],vs,rsk,0);
      unsigned vu=opq(WS_UT+(cb*128+(tid>>3))*4096+(S_-64-t0+(tid&7)*8)*2), lw_=opq(P+8*pc_*128+((15-tg)^pc_)*8);
      middle(IC(15),xc3);
      waitv<1>(pr[0],pr[1],pr[2],pr[3],pr[4],pr[5],pr[6],pr[7],pr[8],pr[9],pr[10],pr[11],pr[12],pr[13],pr[14]);
      #pragma unroll
      for(int e2=0;e2<4;e2++){
        piece(3*e2,1,1); GAP();
        const f32x2 w0={bflo(pr[11+e2][0]),bflo(pr[11+e2][2])}, w1={bfhi(pr[11+e2][0]),bfhi(pr[11+e2][2])}, w2={bflo(pr[11+e2][1]),bflo(pr[11+e2][3])}, w3={bfhi(pr[11+e2][1]),bfhi(pr[11+e2][3])};
        f32x2 kf[7];
        #pragma unroll
        for(int i=0;i<7;i++) kf[i]=f32x2{bflo(pr[i][e2]),bfhi(pr[i][e2])};
        pin(kf[0],kf[3],kf[6]); GAP(); piece(3*e2+1,1,1); GAP();
        f32x2 o[4];
        #pragma unroll
        for(int i=0;i<4;i++){ f32x2 ks=kf[i]*w3; ks=kf[i+1]*w2+ks; ks=kf[i+2]*w1+ks; ks=kf[i+3]*w0+ks; o[i]=ks*f32x2{bflo(pr[7+i][e2]),bfhi(pr[7+i][e2])}; }
        pin(o[0],o[3]); GAP(); piece(3*e2+2,1,1); GAP();
        #pragma unroll
        for(int z=0;z<2;z++){ u32x2 pk={cvtpk(o[3][z],o[2][z]),cvtpk(o[1][z],o[0][z])}; lw64(lw_+(2*e2+z)*128,pk); }
      }
      BARRIER();
      u32x4 v[4];
      #pragma unroll
      for(int i=0;i<4;i++) v[i]=lr128(P+((tid>>3)+32*i)*128+(((tid&7)^((w>>1)+2*i))*16));
      #pragma unroll
      for(int i=0;i<4;i++){ const u32x4 o= (w&1)? u32x4{v[i][2],v[i][3],v[i][0],v[i][1]} : v[i]; st4s<0>(o,vu,rws,i*131072); }
    } else {
      u32x4 xc3[3]; ldx3(xc3);
      u32x4 pr[12];
      const u32x4 rwd=mk_rsrc(wd,F_*D_*2);
      const unsigned vo=(lane>>3)*1280+(lane&7)*16; unsigned so[3], sd[3]; int c4[3];
      #pragma unroll
      for(int z=0;z<3;z++){ const int un=4*g1id+w+256*z, s=un/10, c64=un-10*s; so[z]=rfl(un<540? (32*s*D_+c64*64)*2 : 0x10000000); sd[z]=rfl(un<540? WS_WDF+s*5120 : 0x10000000); c4[z]=4*c64; }
      SFOR(12,i) ld4<0>(pr[i],vo,rwd,so[i>>2]+(i&3)*8*1280); SEND
      middle(IC(12),xc3);
      waitv<1>(pr[0],pr[1],pr[2],pr[3],pr[4],pr[5],pr[6],pr[7],pr[8],pr[9],pr[10],pr[11]);
      const unsigned L=P+w*12288;
      #pragma unroll
      for(int z=0;z<3;z++){
        #pragma unroll
        for(int i=0;i<4;i++){ piece(4*z+i,1,1); const int r=8*i+(lane>>3); lw128(L+z*4096+r*128+(((lane&7)^((r>>2)&7))*16),pr[4*z+i]); } }
      BARRIER();
      const int qq=(lane&15)>>2, p=lane&3, r=4*gq+qq;
      SFOR(12,i) constexpr int z=i>>2, f4=i&3; const int ch=2*f4+(p>>1);
        const u32x4 v=cat(trread(L+z*4096+r*128+((ch^((r>>2)&7))*16)+(p&1)*8),trread(L+z*4096+(r+16)*128+((ch^(((r+16)>>2)&7))*16)+(p&1)*8));
        const int D16=c4[z]+f4, n5=D16/5, dt=D16-5*n5; unsigned so2=rfl(sd[z]+n5*276480+dt*1024); asm volatile("s_nop 4":"+s"(so2)); st4s<0>(v,vw0,rws,so2); SEND
    }
    const int wm=w>>1, wn=w&1;
    unsigned xa[2], wa[4];
    { const int qq=(lane&15)>>2, p=lane&3;
      #pragma unroll
      for(int j=0;j<2;j++) xa[j]=opq(lds0+(64*wm+gj)*128+(((4*j+gq)^((lane>>1)&7))*16));
      #pragma unroll
      for(int z=0;z<4;z++) wa[z]=opq(lds0+16384+(8*gq+qq)*256+(((4*wn+z)^(qq|((gq&1)<<2)))*32)+p*8); }
    f32x4 ag[4][4], au[4][4];
    u32x4 fw[4], fx[2][4];
    u32x4 fp0,fp1; u32x2 fq, st[4]; unsigned csp[2];
    auto rdw=[&](auto GC,auto JC,auto ZC,auto HC) __attribute__((always_inline)) {
      constexpr int G=decltype(GC)::value, J=decltype(JC)::value, Z=decltype(ZC)::value, Hh=decltype(HC)::value;
      const u32x2 t=__builtin_bit_cast(u32x2,trread(wa[Z&3]+J*8192+(Z>>2)*16384+Hh*1024));
      if constexpr(Hh==0){ u32x4 v={t[0],t[1],t[0],t[1]}; fw[G&3]=v; } else { fw[G&3][2]=t[0]; fw[G&3][3]=t[1]; } };
    WAITV(16); BARRIER();
    #pragma unroll
    for(int m=0;m<4;m++) fx[0][m]=lr128(xa[0]+m*2048);
    SFOR(3,z) rdw(IC(z),IC(0),IC(z),IC(0)); rdw(IC(z),IC(0),IC(z),IC(1)); SEND
    SFOR(10,s)
      SFOR(2,j)
        constexpr int c=j&1,n=c^1;
        SFOR(8,z)
          constexpr int G=(2*s+j)*8+z;
          SFOR(4,m)
            f32x4& A= z<4? ag[z&3][m] : au[z&3][m];
            if constexpr(s==0&&j==0) MFMA16Z(A,fw[G&3],fx[c][m]); else MFMA16(A,fw[G&3],fx[c][m]);
            if constexpr(j==0){
              if constexpr(m<2){ if constexpr(z<5) rdw(IC(G+3),IC(0),IC(z+3),IC(m)); else rdw(IC(G+3),IC(1),IC(z-5),IC(m)); }
              if constexpr(m==2 && z>=4) fx[n][z-4]=lr128(xa[1]+(z-4)*2048);
            } else {
              if constexpr(z<5 && m<2) rdw(IC(G+3),IC(1),IC(z+3),IC(m));
              if constexpr(s<9 && z==6){ if constexpr(m<2){ rdw(IC(G+2),IC(0),IC(0),IC(m)); fx[n][m]=lr128(xa[0]+m*2048); } else rdw(IC(G+3),IC(0),IC(1),IC(m-2)); }
              if constexpr(s<9 && z==7 && m<2){ rdw(IC(G+3),IC(0),IC(2),IC(m)); fx[n][2+m]=lr128(xa[0]+(2+m)*2048); }
            }
            if constexpr(s==9 && m==3 && (j==0 || z<4)) ldwd(IC(j*8+z));
            if constexpr(s==0&&j==0){ if constexpr(z<6 && (m&1)) piece(2*z+(m>>1),2,2); }
            else if constexpr(m==3){
              if constexpr(j==1 && z>=6 && s+3<10) piece(z-6,s+3,(s+3)%3);
              if constexpr(j==0 && s+2<10) piece(2+z,s+2,(s+2)%3);
              if constexpr(j==1 && z<2 && s>=1 && s+2<10) piece(10+z,s+2,(s+2)%3);
              if constexpr(s==7 && j==1 && z==2){ const unsigned vo=WS_FP+lane*32; LD4S(fp0,vo,rws,0,0); LD4S(fp1,vo,rws,0,16);
                const unsigned vq=(WS_FQ+lane*8)|(lane<32?0:OOB); LD2S(fq,vq,rws,0,0); }
              if constexpr(s==8 && j==0) conv_piece(ldsC,soC,z);
            }
            GAP();
          SEND
          if constexpr(j==1 && z==5 && s<9){
            if constexpr(s==0) WAITV(16); else if constexpr(s<=6) WAITV(12); else if constexpr(s==7) WAITV(15); else WAITV(8);
            if constexpr(s==8){
              pin(fp0,fp1,fq);
              if(!__all(tag4(fp0,tag)&&tag4(fp1,tag)&&((fq[0]==(unsigned)tag&&fq[1]==(unsigned)(tag>>32))||lane>=32))){
                spin4(rws,WS_FP,64,tag); spin4(rws,WS_FQ,8,tag); }
              if(b==0 && tid==0) put_flag(ws,WS_E,tag);
              #pragma unroll
              for(int m=0;m<4;m++){ const unsigned vo=WS_ST+(128*mi+64*wm+16*m+gj)*8; LD2S(st[m],vo,rws,0,0); }
              const int col= tid<128? ni*128+tid : 1728+ni*128+tid-128; const unsigned vo=(WS_CS+col*4)|(((tid&127)+ni*128<1728)?0:OOB);
              LD1S(csp[0],vo,rws,0,0); { const unsigned so=13824; LD1S(csp[1],vo,rws,so,0); }
              conv_issue(ldsC,chl,2);
            }
            BARRIER();
            if constexpr(s==1) if(tid==0) put_flag(ws,WS_FP+b*8,tag);
            constexpr int dl= (s+1)%3==0? -2*STG : STG;
            xa[0]=opq(xa[0]+dl); xa[1]=opq(xa[1]+dl); wa[0]=opq(wa[0]+dl); wa[1]=opq(wa[1]+dl); wa[2]=opq(wa[2]+dl); wa[3]=opq(wa[3]+dl);
            GAP();
          }
        SEND
      SEND
    SEND
    waitv<14>(csp[0],csp[1],st[0],st[1],st[2],st[3]);
    *(LAS float*)(size_t)(lds0+L_TAB+tid*4)=__uint_as_float(csp[0])+__uint_as_float(csp[1]);
    BARRIER();
    NOP8();
    { const float L2E=1.4426950408889634f;
      f32x4 cg[4], cu[4];
      #pragma unroll
      for(int z=0;z<4;z++){ cg[z]=lr128f(lds0+L_TAB+(64*wn+16*z+4*gq)*4); cu[z]=lr128f(lds0+L_TAB+512+(64*wn+16*z+4*gq)*4); }
      SFOR(4,m)
        const float mean=__uint_as_float(st[m][0]), r=__uint_as_float(st[m][1]);
        const float R=-L2E*r, C=-r*(1.f/L2E), nm=-mean;
        SFOR(2,G)
          u32x4 o;
          SFOR(2,T)
            constexpr int z=2*G+T;
            f32x2 sv[2], uv[2], ex[2];
            #pragma unroll
            for(int e=0;e<2;e++){
              f32x2 g={ag[z][m][2*e],ag[z][m][2*e+1]}, u={au[z][m][2*e],au[z][m][2*e+1]};
              g=f32x2{cg[z][2*e],cg[z][2*e+1]}*nm+g; u=f32x2{cu[z][2*e],cu[z][2*e+1]}*nm+u;
              sv[e]=g*R; ex[e]=f32x2{__builtin_amdgcn_exp2f(sv[e][0]),__builtin_amdgcn_exp2f(sv[e][1])}; uv[e]=(u*C)*sv[e]; }
            pin(ex[0],ex[1],uv[0],uv[1]); GAP();
            ldwd(IC(12+(m*2+G)*4+2*T)); GAP();
            #pragma unroll
            for(int e=0;e<2;e++){ const f32x2 d=ex[e]+1.f; const f32x2 rc={__builtin_amdgcn_rcpf(d[0]),__builtin_amdgcn_rcpf(d[1])};
              const f32x2 h=uv[e]*rc; o[2*T+e]=cvtpk(h[0],h[1]); }
            if constexpr(T==0){ pin(o); GAP(); ldwd(IC(12+(m*2+G)*4+1)); GAP(); }
          SEND
          const int s32=ni*4+2*wn+G, m16=mi*8+wm*4+m;
          const unsigned vo=(WS_HF+((m16*54+s32)*64+lane)*16)|(s32<54?0:OOB); ST4S(o,vo,rws,0,0);
          ldwd(IC(12+(m*2+G)*4+3));
        SEND
      SEND }
    WAITV(40);
  } else {
    {
      const int gw=4*spid+w, cbk=gw>>1, kh=gw&1; const bool act=gw<108;
      const u32x4 rcs=mk_rsrc(cbk<27? wg:wu, D_*F_*2); const int cb64=cbk<27? cbk: cbk-27;
      unsigned vc[2];
      #pragma unroll
      for(int i=0;i<2;i++){ const int r=8*i+(lane>>3), cp=lane&7; const int gs=(cp>>1)^(((r>>1)&1)|((i&1)<<1)); vc[i]=((320*kh+r)*3456+cb64*128+gs*32+(cp&1)*16)|(act?0:OOB); }
      const unsigned LB=rfl(lds0+w*16384);
      auto csissue=[&](int c) __attribute__((always_inline)) {
        #pragma unroll
        for(int i=0;i<8;i++){ SETM0(LB+(c&1)*8192+i*1024); const unsigned so=(64*c+16*(i>>1))*3456; dma<0>(vc[i&1],rcs,so); } };
      u32x4 xc3[3]; ldx3(xc3);
      csissue(0); csissue(1);
      waitv<16>(Eraw,xc3[0],xc3[1],xc3[2]);
      stats(xc3);
      WAITV(0);
      BARRIER();
      if(tid==0) put_flag(ws,WS_FP+b*8,tag);
      const int qq=(lane&15)>>2, p=lane&3; f32x4 cs[4]; u32x4 on={0x3f803f80u,0x3f803f80u,0x3f803f80u,0x3f803f80u}; asm volatile("s_nop 1":"+v"(on));
      SFOR(5,c)
        if constexpr(c>=1 && c<4) WAITV(8); else if constexpr(c==4) WAITV(0);
        u32x4 fa[2][4];
        SFOR(2,k) SFOR(4,z) const int r0=32*k+8*gq+qq; const int g0=((r0>>1)&1)|(((r0>>3)&1)<<1);
          fa[k][z]=cat(trread(LB+(c&1)*8192+r0*128+((z^g0)*32)+p*8),trread(LB+(c&1)*8192+(r0+4)*128+((z^g0)*32)+p*8)); SEND SEND
        WAITL();
        if constexpr(c+2<5) csissue(c+2);
        GAP();
        SFOR(2,k) SFOR(4,z) if constexpr(c==0&&k==0) MFMA16Z(cs[z],fa[k][z],on); else MFMA16(cs[z],fa[k][z],on); SEND SEND
        GAP();
      SEND
      NOP8(); pin(cs[0],cs[1],cs[2],cs[3]);
      f32x4 v;
      #pragma unroll
      for(int e=0;e<4;e++) v[e]= gj==0? cs[0][e] : gj==1? cs[1][e] : gj==2? cs[2][e] : cs[3][e];
      const unsigned vo=(WS_CS+(kh*3456+cbk*64+16*gj+4*gq)*4)|((act&&gj<4)?0:OOB); ST4S(__builtin_bit_cast(u32x4,v),vo,rws,0,0);
    }
    WAITV(0); BARRIER();
    if(tid==0) put_flag(ws,WS_FQ+spid*8,tag);
    conv_issue(ldsR0,4*spid+cvp,0); conv_issue(ldsR0,4*spid+cvp,1);
    conv_issue(ldsR1,4*spid+2+cvp,0); conv_issue(ldsR1,4*spid+2+cvp,1);
    conv_issue(ldsC,chl,0); conv_issue(ldsC,chl,1);
  }
  auto dof=[&](int ch) __attribute__((always_inline)) ->float { return bflo(((const unsigned*)Dv)[ch>>1]>>((ch&1)*16)); };
  u32x2 ph, xv[5], stv; u32x4 WV[6];
  const int trow=64*mt+16*w+gj;
  u32x4 H[14][4];
  unsigned vh[4];
  #pragma unroll
  for(int r=0;r<4;r++) vh[r]=WS_HF+((w+r)&3)*54*1024+lane*16;
  auto hoff=[&](int i) __attribute__((always_inline)) ->unsigned { return i<13? hbase+i*1024 : h13; };
  auto ldH=[&](int i) __attribute__((always_inline)) { const unsigned sh=hoff(i);
    LD4S(H[i][0],vh[0],rws,sh,0); LD4S(H[i][1],vh[1],rws,sh,0); LD4S(H[i][2],vh[2],rws,sh,0); LD4S(H[i][3],vh[3],rws,sh,0); };
  auto conv_round=[&](auto LASTC, auto TBC, const unsigned base, const int ch, const float Dd) __attribute__((always_inline)) {
    constexpr bool LAST=decltype(LASTC)::value; constexpr int TB=decltype(TBC)::value;
    BARRIER();
    {
      const int c=lane>>4,p=lane&15; const LAS u16* src=(const LAS u16*)(size_t)(base+32+ (p>=16-c? (p-16+c)*2:0));
      u16 v=*src; *(LAS u16*)(size_t)(base+c*CV_HC+p*2)= p>=16-c? v:(u16)0;
      if(lane<32) lw128(base+CV_H+4096+lane*16,u32x4{0,0,0,0}); }
    WAITL();
    const int ci=gj,cq=gq,cc=(ci+1)&3;
    const unsigned pa=opq(base+cc*CV_HC+2*(1+ci-cc+8*cq)+256*cvh), pb=opq(base+CV_H+2*(2032-16*ci+8*cq)-3584+256*cvh);
    f32x4 ya[8];
    { u32x4 B[2][8], A[2][8];
      auto ldA=[&](int rho,int a)->u32x4{ const unsigned p=pa+64*rho+512*a; return cat2(lr64v(p),lr64v(p+8)); };
      auto ldB=[&](int rho,int bb)->u32x4{ return lr128(pb+64*rho+512*(7-bb)); };
      #pragma unroll
      for(int z=0;z<8;z++){ B[0][z]=ldB(0,z); A[0][z]=ldA(0,z); }
      GAP();
      SFOR(4,rho)
        constexpr int c=rho&1,n=c^1;
        if constexpr(LAST && rho==1){
          WAITV(9); if(isg && lane==0) put_flag(ws,WS_FH+(mi*64+ni*4+w)*8,tag); }
        SFOR(8,pos)
          constexpr int a= (pos&1)? 7-(pos>>1) : (pos>>1);
          constexpr int c0= pos==0?0: pos==1?8: pos==2?9: pos==3?16: pos==4?18: pos==5?24: pos==6?27: 32;
          SFOR(8-a,nn_)
            constexpr int nu= pos==0? 7-nn_ : pos==7? (nn_<2? 4+nn_ : 9-nn_) : a+nn_; constexpr int cnt=c0+nn_;
            if constexpr(rho==0 && pos==0) MFMA16Z(ya[nu],A[c][a],B[c][nu-a]); else MFMA16(ya[nu],A[c][a],B[c][nu-a]);
            if constexpr(rho<3){
              if constexpr(cnt<8) B[n][cnt]=ldB(rho+1,cnt);
              else if constexpr(cnt<24){ constexpr int az=(cnt-8)>>1; const u32x2 t=lr64v(pa+64*(rho+1)+512*az+8*(cnt&1));
                if constexpr((cnt&1)==0){ u32x4 v={t[0],t[1],t[0],t[1]}; A[n][az]=v; } else { A[n][az][2]=t[0]; A[n][az][3]=t[1]; } } }
            if constexpr(LAST && cnt%4==3 && cnt<32){ constexpr int q=rho*8+cnt/4;
              if constexpr(q<20) ldwd(IC(NWE+q));
              else if constexpr(q<26){ constexpr int z=q-20, n2=64+z, i2=n2/5, d2=n2%5; const unsigned so=(i2<13? wdbase+i2*5120 : wd13)+d2*1024; ld4s<0>(WV[z],vw0,rws,so); }
              else if constexpr(q<31){ const unsigned vo=(trow*D_+80*nt+4*gq)*2; ld2<32*(q-26)>(xv[q-26],vo,rx,0); }
              else { const unsigned vs=WS_ST+trow*8; LD2S(stv,vs,rws,0,0); } }
            if constexpr(TB>=0 && cnt%4==3 && cnt<32){ constexpr int q=TB+rho*8+cnt/4; if constexpr(q<NWE) ldwd(IC(q)); }
            GAP();
          SEND
        SEND
      SEND
    }
    if constexpr(LAST){
      const unsigned vo=(WS_FH+((mt>>1)*64+lane)*8)|(lane<56?0:OOB); LD2S(ph,vo,rws,0,0); }
    NOP8();
    f32x4 mine[4];
    { const unsigned R=lds0+L_RED+w*4096+lane*16;
      #pragma unroll
      for(int z=0;z<4;z++){ const f32x4 lo=ya[z], hi=ya[4+z]; f32x4 snd, kp;
        #pragma unroll
        for(int e=0;e<4;e++){ snd[e]= cvh? lo[e]:hi[e]; kp[e]= cvh? hi[e]:lo[e]; }
        lw128f(R+z*1024,snd); mine[z]=kp; } }
    BARRIER();
    { const unsigned R=lds0+L_RED+(w^1)*4096+lane*16; unsigned pk[4][2];
      #pragma unroll
      for(int z=0;z<4;z++){ const f32x4 y=mine[z]+lr128f(R+z*1024);
        const int t=256*(4*cvh+z)+16*ci+4*cq;
        const u32x2 uu=*(const LAS u32x2*)(size_t)(base+CV_H+2*(2044-t));
        pk[z][0]=cvtpk(fmaf(bfhi(uu[1]),Dd,y[0]),fmaf(bflo(uu[1]),Dd,y[1])); pk[z][1]=cvtpk(fmaf(bfhi(uu[0]),Dd,y[2]),fmaf(bflo(uu[0]),Dd,y[3])); }
      #pragma unroll
      for(int z=0;z<4;z+=2){
        auto r0=__builtin_amdgcn_permlane16_swap(pk[z][0],pk[z+1][0],false,false);
        auto r1=__builtin_amdgcn_permlane16_swap(pk[z][1],pk[z+1][1],false,false);
        u32x4 v={r0[0],r1[0],r0[1],r1[1]};
        const int t=256*(4*cvh+z+(cq&1))+16*ci+8*(cq>>1);
        const unsigned vo=WS_YT+ch*4096+t*2; ST4S(v,vo,rws,0,0); }
    }
    if constexpr(LAST){
      waitv<2>(ph);
      if(!__all((ph[0]==(unsigned)tag && ph[1]==(unsigned)(tag>>32)) || lane>=56)) spin4(rws,WS_FH+(mt>>1)*512,14,tag);
      ldH(0); ldH(1); ldH(2); ldH(3); }
  };
  if(!isg){
    spin4(rws,WS_FP,64,tag);
    conv_issue(ldsR0,4*spid+cvp,2); conv_issue(ldsR1,4*spid+2+cvp,2); conv_issue(ldsC,chl,2);
    WAITV(4);
    conv_round(std::integral_constant<bool,false>{},IC(0),ldsR0,4*spid+cvp,dof(4*spid+cvp));
    WAITV(36);
    conv_round(std::integral_constant<bool,false>{},IC(32),ldsR1,4*spid+2+cvp,dof(4*spid+2+cvp));
    WAITV(0);
    if(lane==0){ put_flag(ws,WS_FY+((4*spid+cvp)*2+cvh)*8,tag); put_flag(ws,WS_FY+((4*spid+2+cvp)*2+cvh)*8,tag); }
  }
  conv_round(std::integral_constant<bool,true>{},IC(-1),ldsC,chl,DdC);
  u32x4 py0,py1; u32x4 yv[3];
  f32x4 oa[4][5];
  #define HREGS(i) H[i][0],H[i][1],H[i][2],H[i][3]
  SFOR(14,i)
    if constexpr(i==0){ waitv<12>(HREGS(0),xv[0],xv[1],xv[2],xv[3],xv[4],stv,WV[0],WV[1],WV[2],WV[3],WV[4],WV[5]);
                        if(lane==0) put_flag(ws,WS_FY+(chl*2+cvh)*8,tag); }
    else if constexpr(i<=10) waitv<12>(HREGS(i));
    else if constexpr(i==11) waitv<10>(HREGS(11));
    else if constexpr(i==12){ waitv<0>(HREGS(12),HREGS(13),py0,py1);
      if(!__all((tag4(py0,tag)&&tag4(py1,tag))||lane>=40)) spin4(rws,WS_FY+160*nt*8,40,tag); }
    GAP();
    SFOR(4,tt)
      SFOR(5,dt)
        constexpr int nn=5*i+dt, sl=tt*5+dt;
        if constexpr(i==0) mfmawz<nn>(oa[tt][dt],H[0][tt]);
        else if constexpr(nn<64) mfmaw<nn>(oa[tt][dt],H[i][tt]);
        else MFMA16(oa[tt][dt],WV[nn-64],H[i][tt]);
        if constexpr(i+4<14 && sl%5==1){ const unsigned sh=hoff(i+4); ld4s<0>(H[i+4][sl/5],vh[sl/5],rws,sh); }
        if constexpr(i==10 && sl==2){ const unsigned vo=(WS_FY+(160*nt+4*lane)*8)|(lane<40?0:OOB); LD4S(py0,vo,rws,0,0); LD4S(py1,vo,rws,0,16); }
        if constexpr(i==12 && sl%5==1 && sl<15){ constexpr int z=sl/5; const int id=tid+256*z; const int r=id>>3,c8=id&7;
          const unsigned vo=(WS_YT+(80*nt+r)*4096+(64*mt+c8*8)*2)|(id<640?0:OOB); LD4S(yv[z],vo,rws,0,0); }
        GAP();
      SEND
    SEND
  SEND
  NOP8();
  #pragma unroll
  for(int r=1;r<4;r++){ const unsigned a=lds0+((((w+r)&3)*3+(r-1))*5)*1024+lane*16;
    #pragma unroll
    for(int dt=0;dt<5;dt++) lw128f(a+dt*1024,oa[r][dt]); }
  waitv<0>(yv[0],yv[1],yv[2]);
  #pragma unroll
  for(int z=0;z<3;z++){ const int id=tid+256*z; if(id<640) lw128(lds0+L_Y+(id>>3)*160+(id&7)*16,yv[z]); }
  BARRIER();
  { const float m=__uint_as_float(stv[0]), rd=__uint_as_float(stv[1]); const unsigned a=lds0+(w*15)*1024+lane*16;
    #pragma unroll
    for(int dt=0;dt<5;dt++){
      f32x4 f=(oa[0][dt]+lr128f(a+dt*1024))+(lr128f(a+(5+dt)*1024)+lr128f(a+(10+dt)*1024));
      const u32x2 y2=__builtin_bit_cast(u32x2,trread(lds0+L_Y+(16*dt+4*gq+((lane&15)>>2))*160+(16*w+4*(lane&3))*2));
      const float xf[4]={bflo(xv[dt][0]),bfhi(xv[dt][0]),bflo(xv[dt][1]),bfhi(xv[dt][1])};
      const float yf[4]={bflo(y2[0]),bfhi(y2[0]),bflo(y2[1]),bfhi(y2[1])};
      float o[4];
      #pragma unroll
      for(int e=0;e<4;e++) o[e]=fmaf((xf[e]-m)*rd,yf[e],xf[e])+f[e];
      u32x2 ov={cvtpk(o[0],o[1]),cvtpk(o[2],o[3])};
      *(u32x2*)(out+(size_t)trow*D_+80*nt+16*dt+4*gq)=ov;
    } }
}

extern "C" void kernel_launch(void* const* d_in, const int* in_sizes, int n_in, void* d_out, int out_size, void* d_ws, size_t ws_size, hipStream_t stream){
  (void)hipFuncSetAttribute((const void*)fused_kernel,hipFuncAttributeMaxDynamicSharedMemorySize,LDS_BYTES);
  fused_kernel<<<256,256,LDS_BYTES,stream>>>((const u16*)d_in[0],(const u16*)d_in[1],(const u16*)d_in[2],(const u16*)d_in[3],(const u16*)d_in[4],(const u16*)d_in[5],
      (const u16*)d_in[7],(const u16*)d_in[8],(const u16*)d_in[9],(u16*)d_out,(char*)d_ws);
}
```
